```python
import torch

def _softmax(x, axis=-1):
    return torch.softmax(x, dim=axis)
def _gelu(x):
    return torch.nn.functional.gelu(x, approximate="tanh")
def _silu(x):
    return torch.nn.functional.silu(x)
def _sigmoid(x):
    return torch.sigmoid(x)
def _layernorm(x, axis=-1, eps=1e-5):
    m = x.mean(dim=axis, keepdim=True)
    v = x.var(dim=axis, keepdim=True, unbiased=False)
    return (x - m) / torch.sqrt(v + eps)
def _scatter_add(tgt, idx, src, axis=0):
    return tgt.clone().index_add_(axis, idx, src)
def _segment_sum(src, seg, n):
    out = torch.zeros((n,) + src.shape[1:], dtype=src.dtype)
    return out.index_add_(0, seg, src)
def _pad(x, pw):
    flat = tuple(v for pair in reversed(pw) for v in pair)
    return torch.nn.functional.pad(x, flat)

kernel_name = 'gated_mix__gqa_block_cached__gqa_block_cached__decode'

def setup_inputs():

    _w = (lambda a: (lambda t: t.to(torch.bfloat16) if t.is_floating_point() else t)(torch.as_tensor(a)))
    def _A_setup():
        B, Sq, D = 2, 1, 448
        Hq, Hkv, Sk, Dh = 16, 8, 8192, 256
        assert Hq % Hkv == 0, (Hq, Hkv)
        return {
            "x": _w((lambda *s: torch.randn(*s, dtype=torch.bfloat16) * 0.05)(B, Sq, D)),
            "k_cache": _w((lambda *s: torch.randn(*s, dtype=torch.bfloat16) * 6.4)(B, Hkv, Sk, Dh)),
            "v_cache": _w((lambda *s: torch.randn(*s, dtype=torch.bfloat16) * 0.05)(B, Hkv, Sk, Dh)),
            "wq": _w((lambda *s: torch.randn(*s, dtype=torch.bfloat16) * 6.4)(D, Hq * Dh) * (D ** -0.5)),
            "wo": _w((lambda *s: torch.randn(*s, dtype=torch.bfloat16) * 0.05)(Hq * Dh, D) * ((Hq * Dh) ** -0.5)),
        }
    _A_in = _A_setup()

    def _B_setup():
        B, Sq, D = 2, 1, 448
        Hq, Hkv, Sk, Dh = 16, 8, 8192, 256
        assert Hq % Hkv == 0, (Hq, Hkv)
        return {
            "x": _w((lambda *s: torch.randn(*s, dtype=torch.bfloat16) * 0.05)(B, Sq, D)),
            "k_cache": _w((lambda *s: torch.randn(*s, dtype=torch.bfloat16) * 6.4)(B, Hkv, Sk, Dh)),
            "v_cache": _w((lambda *s: torch.randn(*s, dtype=torch.bfloat16) * 0.05)(B, Hkv, Sk, Dh)),
            "wq": _w((lambda *s: torch.randn(*s, dtype=torch.bfloat16) * 6.4)(D, Hq * Dh) * (D ** -0.5)),
            "wo": _w((lambda *s: torch.randn(*s, dtype=torch.bfloat16) * 0.05)(Hq * Dh, D) * ((Hq * Dh) ** -0.5)),
        }
    _B_in = _B_setup()

    return {
        "A_x": _A_in["x"],
        "A_k_cache": _A_in["k_cache"],
        "A_v_cache": _A_in["v_cache"],
        "A_wq": _A_in["wq"],
        "A_wo": _A_in["wo"],
        "B_x": _B_in["x"],
        "B_k_cache": _B_in["k_cache"],
        "B_v_cache": _B_in["v_cache"],
        "B_wq": _B_in["wq"],
        "B_wo": _B_in["wo"],
        "gate_w": _w((lambda *s: torch.randn(*s, dtype=torch.bfloat16) * 0.05)(448) * 16.0),
    }

def reference(A_x, A_k_cache, A_v_cache, A_wq, A_wo, B_x, B_k_cache, B_v_cache, B_wq, B_wo, gate_w):
    def _A_ref(x, k_cache, v_cache, wq, wo):
        B, Sq, D = x.shape
        Hq, Dh = 16, 256
        Hkv = k_cache.shape[1]
        rep = Hq // Hkv
        q = (lambda x, *d: x.permute(*d))((x @ wq).reshape(B, Sq, Hq, Dh), 0, 2, 1, 3)
        k = (lambda x, n, axis: torch.repeat_interleave(x, n, dim=axis))(k_cache, rep, axis=1)
        v = (lambda x, n, axis: torch.repeat_interleave(x, n, dim=axis))(v_cache, rep, axis=1)
        scores = torch.einsum("bhqd,bhkd->bhqk", q, k) * (Dh ** -0.5)
        p = _softmax(scores, axis=-1)
        o = torch.einsum("bhqk,bhkd->bhqd", p, v)
        return (lambda x, *d: x.permute(*d))(o, 0, 2, 1, 3).reshape(B, Sq, Hq * Dh) @ wo

    def _B_ref(x, k_cache, v_cache, wq, wo):
        B, Sq, D = x.shape
        Hq, Dh = 16, 256
        Hkv = k_cache.shape[1]
        rep = Hq // Hkv
        q = (lambda x, *d: x.permute(*d))((x @ wq).reshape(B, Sq, Hq, Dh), 0, 2, 1, 3)
        k = (lambda x, n, axis: torch.repeat_interleave(x, n, dim=axis))(k_cache, rep, axis=1)
        v = (lambda x, n, axis: torch.repeat_interleave(x, n, dim=axis))(v_cache, rep, axis=1)
        scores = torch.einsum("bhqd,bhkd->bhqk", q, k) * (Dh ** -0.5)
        p = _softmax(scores, axis=-1)
        o = torch.einsum("bhqk,bhkd->bhqd", p, v)
        return (lambda x, *d: x.permute(*d))(o, 0, 2, 1, 3).reshape(B, Sq, Hq * Dh) @ wo

    x = A_x
    g = _sigmoid((x * gate_w).sum(-1, keepdims=True))
    a = _A_ref(x, A_k_cache, A_v_cache, A_wq, A_wo)
    b = _B_ref(x, B_k_cache, B_v_cache, B_wq, B_wo)
    return g * a + (1 - g) * b
```

```cpp
#include <hip/hip_runtime.h>
#include <hip/hip_bf16.h>
#include <stdint.h>

typedef unsigned u32x4 __attribute__((ext_vector_type(4)));
typedef unsigned u32x2 __attribute__((ext_vector_type(2)));
typedef float f32x4 __attribute__((ext_vector_type(4)));
typedef short bf16x8 __attribute__((ext_vector_type(8)));
typedef __bf16 bf16x2 __attribute__((ext_vector_type(2)));

constexpr int NB = 2, DM = 448, HQ = 16, HKV = 8, SK = 8192, DH = 256;
constexpr int NSPLIT = 8, NWAVE = 8, KEYS_WG = SK / NSPLIT, KEYS_WAVE = KEYS_WG / NWAVE;
constexpr int RING = 32;

__device__ __forceinline__ float dot2(unsigned a, unsigned b, float acc) {
  return __builtin_amdgcn_fdot2_f32_bf16(__builtin_bit_cast(bf16x2, a), __builtin_bit_cast(bf16x2, b), acc, false);
}
template <int CTRL> __device__ __forceinline__ float dpp(float x) {
  return __builtin_bit_cast(float, __builtin_amdgcn_mov_dpp(__builtin_bit_cast(int, x), CTRL, 0xf, 0xf, true));
}
constexpr int XOR1 = 0xB1, XOR2 = 0x4E, XOR7 = 0x141, XOR8 = 0x128;
__device__ __forceinline__ float bf_lo(unsigned u) { return __builtin_bit_cast(float, u << 16); }
__device__ __forceinline__ float bf_hi(unsigned u) { return __builtin_bit_cast(float, u & 0xffff0000u); }
__device__ __forceinline__ unsigned short f2bf(float f) {
  __bf16 h = (__bf16)f;
  return __builtin_bit_cast(unsigned short, h);
}
__device__ __forceinline__ float swap16_add(float a, float b) {
  auto r = __builtin_amdgcn_permlane16_swap(__builtin_bit_cast(unsigned, a), __builtin_bit_cast(unsigned, b), false, false);
  return __builtin_bit_cast(float, (unsigned)r[0]) + __builtin_bit_cast(float, (unsigned)r[1]);
}
__device__ __forceinline__ float swap32_add(float a, float b) {
  auto r = __builtin_amdgcn_permlane32_swap(__builtin_bit_cast(unsigned, a), __builtin_bit_cast(unsigned, b), false, false);
  return __builtin_bit_cast(float, (unsigned)r[0]) + __builtin_bit_cast(float, (unsigned)r[1]);
}
__device__ __forceinline__ float swap32_max(float a) {
  auto r = __builtin_amdgcn_permlane32_swap(__builtin_bit_cast(unsigned, a), __builtin_bit_cast(unsigned, a), false, false);
  return fmaxf(__builtin_bit_cast(float, (unsigned)r[0]), __builtin_bit_cast(float, (unsigned)r[1]));
}
template <int CTRL> __device__ __forceinline__ float tr_step(float lo, float hi, bool sel) {
  float t1 = lo + dpp<CTRL>(lo), t2 = hi + dpp<CTRL>(hi);
  return sel ? t2 : t1;
}

struct Ptrs {
  const unsigned short* x;
  const unsigned short* k[2];
  const unsigned short* v[2];
  const unsigned short* wq[2];
  const unsigned short* wo[2];
  const unsigned short* gw;
  unsigned short* out;
  unsigned char* ws;
};
constexpr size_t WS_Q = 0;
constexpr size_t WS_ML = 65536;
constexpr size_t WS_O = 131072;
constexpr size_t WS_Y = WS_O + 256 * 512 * 4;

__device__ __forceinline__ void decode_wg(int bid, int& br, int& b, int& kvh, int& s) {
  int xcd = bid & 7, r = bid >> 3;
  int combo = xcd * 2 + (r >> 4);
  int j = r & 15;
  br = combo >> 3; kvh = combo & 7; b = j >> 3; s = j & 7;
}

__global__ void __launch_bounds__(512) k_qproj(Ptrs p) {
  __shared__ float red[7][64];
  int br, b, kvh, s; decode_wg(blockIdx.x, br, b, kvh, s);
  const int j = b * 8 + s;
  const int tid = threadIdx.x, lane = tid & 63, wave = tid >> 6;
  const int u = tid >> 2, c4 = tid & 3;
  if (wave < 7) {
    const unsigned short* wq = p.wq[br] + (size_t)(4 * u) * 4096 + kvh * 512 + j * 32 + c4 * 8;
    u32x4 w[4];
#pragma unroll
    for (int i = 0; i < 4; ++i) w[i] = *(const u32x4*)(wq + (size_t)i * 4096);
    u32x2 x0 = *(const u32x2*)(p.x + 4 * u), x1 = *(const u32x2*)(p.x + DM + 4 * u);
    float xf[2][4] = {{bf_lo(x0[0]), bf_hi(x0[0]), bf_lo(x0[1]), bf_hi(x0[1])}, {bf_lo(x1[0]), bf_hi(x1[0]), bf_lo(x1[1]), bf_hi(x1[1])}};
    float a[16];
#pragma unroll
    for (int e = 0; e < 16; ++e) a[e] = 0.f;
#pragma unroll
    for (int i = 0; i < 4; ++i)
#pragma unroll
      for (int e = 0; e < 4; ++e) {
        float lo = bf_lo(w[i][e]), hi = bf_hi(w[i][e]);
        a[2 * e] = fmaf(xf[0][i], lo, a[2 * e]); a[2 * e + 1] = fmaf(xf[0][i], hi, a[2 * e + 1]);
        a[8 + 2 * e] = fmaf(xf[1][i], lo, a[8 + 2 * e]); a[8 + 2 * e + 1] = fmaf(xf[1][i], hi, a[8 + 2 * e + 1]);
      }
#pragma unroll
    for (int e = 0; e < 8; ++e) a[e] = swap32_add(a[e], a[e + 8]);
#pragma unroll
    for (int e = 0; e < 4; ++e) a[e] = swap16_add(a[e], a[e + 4]);
#pragma unroll
    for (int e = 0; e < 2; ++e) a[e] = tr_step<XOR8>(a[e], a[e + 2], lane & 8);
    {
      float lo = a[0], hi = a[1];
      float lo2 = dpp<0x1B>(dpp<XOR7>(lo)), hi2 = dpp<0x1B>(dpp<XOR7>(hi));
      a[0] = (lane & 4) ? hi + hi2 : lo + lo2;
    }
    red[wave][lane] = a[0];
  }
  __syncthreads();
  if (wave == 0) {
    float v = 0.f;
#pragma unroll
    for (int w = 0; w < 7; ++w) v += red[w][lane];
    int bb = lane >> 5, e = ((lane >> 4) & 1) * 4 + ((lane >> 3) & 1) * 2 + ((lane >> 2) & 1), col = (lane & 3) * 8 + e;
    unsigned short* q = (unsigned short*)(p.ws + WS_Q);
    q[(br * 2 + bb) * 4096 + kvh * 512 + j * 32 + col] = f2bf(v);
  }
}

__global__ void __launch_bounds__(512, 2) k_attn(Ptrs p) {
  __shared__ __attribute__((aligned(16))) unsigned pl[NWAVE][2][4][32];
  __shared__ __attribute__((aligned(16))) float ol[NWAVE][512];
  __shared__ float ml[NWAVE][2][2];
  int br, b, kvh, s; decode_wg(blockIdx.x, br, b, kvh, s);
  const int tid = threadIdx.x, lane = tid & 63;
  const int wave = __builtin_amdgcn_readfirstlane(tid >> 6);
  const size_t base = ((size_t)(b * HKV + kvh) * SK + s * KEYS_WG + wave * KEYS_WAVE) * DH;
  __amdgpu_buffer_rsrc_t rk = __builtin_amdgcn_make_buffer_rsrc((void*)(p.k[br] + base), 0, KEYS_WAVE * DH * 2, 0x00020000);
  __amdgpu_buffer_rsrc_t rv = __builtin_amdgcn_make_buffer_rsrc((void*)(p.v[br] + base), 0, KEYS_WAVE * DH * 2, 0x00020000);
  const int koff = lane * 16;
  const int voff = (lane >> 4) * 512 + (lane & 15) * 16;
  auto ld = [&](int i) -> u32x4 {
    if (i < 64) return __builtin_amdgcn_raw_buffer_load_b128(rk, koff, i * 1024, 2);
    int v = i - 64;
    return __builtin_amdgcn_raw_buffer_load_b128(rv, voff, (v >> 1) * 2048 + (v & 1) * 256, 2);
  };
  const unsigned short* q = (const unsigned short*)(p.ws + WS_Q) + (br * 2 + b) * 4096 + kvh * 512 + (lane & 31) * 8;
  u32x4 q0 = *(const u32x4*)q, q1 = *(const u32x4*)(q + 256);
  u32x4 ring[RING];
#pragma unroll
  for (int i = 0; i < RING; ++i) ring[i] = ld(i);
  __builtin_amdgcn_sched_barrier(0);
  float sc[4];
  float t0 = 0.f, t1 = 0.f, t2 = 0.f, t3 = 0.f;
#pragma unroll
  for (int i = 0; i < 64; ++i) {
    u32x4 kk = ring[i % RING];
    float x0 = 0.f, x1 = 0.f;
#pragma unroll
    for (int e = 0; e < 4; ++e) { x0 = dot2(kk[e], q0[e], x0); x1 = dot2(kk[e], q1[e], x1); }
    ring[i % RING] = ld(i + RING);
    float y = swap16_add(x0, x1);
    if ((i & 1) == 0) t0 = y;
    else {
      y = tr_step<XOR8>(t0, y, lane & 8);
      if ((i & 2) == 0) t1 = y;
      else {
        y = tr_step<XOR7>(t1, y, lane & 4);
        if ((i & 4) == 0) t2 = y;
        else {
          y = tr_step<XOR2>(t2, y, lane & 2);
          if ((i & 8) == 0) t3 = y;
          else { y = tr_step<XOR1>(t3, y, lane & 1); sc[i >> 4] = y; }
        }
      }
    }
    __builtin_amdgcn_sched_barrier(0);
  }
  constexpr float C2 = 0.0625f * 1.4426950408889634f;
  float m = fmaxf(fmaxf(sc[0], sc[1]), fmaxf(sc[2], sc[3]));
  m = fmaxf(m, dpp<XOR1>(m)); m = fmaxf(m, dpp<XOR2>(m)); m = fmaxf(m, dpp<XOR7>(m)); m = fmaxf(m, dpp<XOR8>(m));
  m = swap32_max(m);
  const float mc = m * C2;
  float l = 0.f;
  {
    const int par = lane >> 5, h = (lane >> 4) & 1;
    const int u = ((lane >> 3) & 1) | (((lane >> 2) & 1) << 1) | (((lane >> 1) & 1) << 2) | ((lane & 1) << 3);
#pragma unroll
    for (int t = 0; t < 4; ++t) {
      float pv = __builtin_amdgcn_exp2f(fmaf(sc[t], C2, -mc));
      l += pv;
      unsigned pb = f2bf(pv);
      int key = 2 * (16 * t + u) + par;
      pl[wave][h][key & 3][key >> 2] = pb | (pb << 16);
    }
  }
  l += dpp<XOR1>(l); l += dpp<XOR2>(l); l += dpp<XOR7>(l); l += dpp<XOR8>(l);
  l = swap32_add(l, l);
  if ((lane & 47) == 0) { ml[wave][(lane >> 4) & 1][0] = mc; ml[wave][(lane >> 4) & 1][1] = l; }
  __builtin_amdgcn_fence(__ATOMIC_RELEASE, "wavefront");
  __builtin_amdgcn_wave_barrier();
  __builtin_amdgcn_fence(__ATOMIC_ACQUIRE, "wavefront");
  unsigned msk[4];
#pragma unroll
  for (int r = 0; r < 4; ++r) msk[r] = (((lane & 7) >> 1) == r) ? ((lane & 1) ? 0xffff0000u : 0x0000ffffu) : 0u;
  const unsigned* prow = &pl[wave][(lane >> 3) & 1][lane >> 4][0];
  f32x4 acc0 = {0.f, 0.f, 0.f, 0.f}, acc1 = {0.f, 0.f, 0.f, 0.f};
  u32x4 pq;
#pragma unroll
  for (int j = 0; j < 32; ++j) {
    if ((j & 3) == 0) pq = *(const u32x4*)(prow + j);
    unsigned pd = pq[j & 3];
    u32x4 a = {pd & msk[0], pd & msk[1], pd & msk[2], pd & msk[3]};
    const int i0 = 64 + 2 * j, i1 = i0 + 1;
    acc0 = __builtin_amdgcn_mfma_f32_16x16x32_bf16(__builtin_bit_cast(bf16x8, a), __builtin_bit_cast(bf16x8, ring[i0 % RING]), acc0, 0, 0, 0);
    if (i0 + RING < 128) ring[i0 % RING] = ld(i0 + RING);
    acc1 = __builtin_amdgcn_mfma_f32_16x16x32_bf16(__builtin_bit_cast(bf16x8, a), __builtin_bit_cast(bf16x8, ring[i1 % RING]), acc1, 0, 0, 0);
    if (i1 + RING < 128) ring[i1 % RING] = ld(i1 + RING);
    __builtin_amdgcn_sched_barrier(0);
  }
  {
    const int rg = lane >> 4, h = rg >> 1, e0 = (rg & 1) * 4, c = lane & 15;
    *(f32x4*)&ol[wave][h * 256 + 8 * c + e0] = acc0;
    *(f32x4*)&ol[wave][h * 256 + 128 + 8 * c + e0] = acc1;
  }
  __syncthreads();
  {
    const int h = tid >> 8;
    float M = ml[0][h][0];
#pragma unroll
    for (int w = 1; w < NWAVE; ++w) M = fmaxf(M, ml[w][h][0]);
    float o = 0.f, L = 0.f;
#pragma unroll
    for (int w = 0; w < NWAVE; ++w) {
      float sw = __builtin_amdgcn_exp2f(ml[w][h][0] - M);
      o = fmaf(sw, ol[w][tid], o); L = fmaf(sw, ml[w][h][1], L);
    }
    const int g = ((br * 2 + b) * 8 + kvh) * 8 + s;
    ((float*)(p.ws + WS_O))[g * 512 + tid] = o;
    if ((tid & 255) == 0) { float* mlp = (float*)(p.ws + WS_ML) + (g * 2 + h) * 2; mlp[0] = M; mlp[1] = L; }
  }
}

__global__ void __launch_bounds__(512) k_comb(Ptrs p) {
  __shared__ __attribute__((aligned(16))) float os[64];
  int br, b, kvh, s; decode_wg(blockIdx.x, br, b, kvh, s);
  const int tid = threadIdx.x, lane = tid & 63, wave = tid >> 6;
  const int g0 = ((br * 2 + b) * 8 + kvh) * 8;
  const int h = s >> 2;
  u32x4 w[8];
  if (wave < 7) {
    const unsigned short* wo = p.wo[br] + (size_t)(kvh * 512 + s * 64 + (lane >> 3) * 8) * DM + wave * 64 + (lane & 7) * 8;
#pragma unroll
    for (int i = 0; i < 8; ++i) w[i] = *(const u32x4*)(wo + i * DM);
  } else {
    const float* mlp = (const float*)(p.ws + WS_ML);
    float M = -INFINITY;
#pragma unroll
    for (int t = 0; t < 8; ++t) M = fmaxf(M, mlp[((g0 + t) * 2 + h) * 2]);
    float o = 0.f, L = 0.f;
#pragma unroll
    for (int t = 0; t < 8; ++t) {
      float sw = __builtin_amdgcn_exp2f(mlp[((g0 + t) * 2 + h) * 2] - M);
      L = fmaf(sw, mlp[((g0 + t) * 2 + h) * 2 + 1], L);
      o = fmaf(sw, ((const float*)(p.ws + WS_O))[(g0 + t) * 512 + s * 64 + lane], o);
    }
    os[lane] = o / L;
  }
  __syncthreads();
  if (wave < 7) {
    float a[8];
#pragma unroll
    for (int e = 0; e < 8; ++e) a[e] = 0.f;
#pragma unroll
    for (int i = 0; i < 8; ++i) {
      float o = os[(lane >> 3) * 8 + i];
#pragma unroll
      for (int e = 0; e < 4; ++e) { a[2 * e] = fmaf(o, bf_lo(w[i][e]), a[2 * e]); a[2 * e + 1] = fmaf(o, bf_hi(w[i][e]), a[2 * e + 1]); }
    }
#pragma unroll
    for (int e = 0; e < 4; ++e) a[e] = swap32_add(a[e], a[e + 4]);
#pragma unroll
    for (int e = 0; e < 2; ++e) a[e] = swap16_add(a[e], a[e + 2]);
    a[0] = tr_step<XOR8>(a[0], a[1], lane & 8);
    const int e = ((lane >> 5) & 1) * 4 + ((lane >> 4) & 1) * 2 + ((lane >> 3) & 1);
    ((float*)(p.ws + WS_Y))[(g0 + s) * DM + wave * 64 + (lane & 7) * 8 + e] = a[0];
  }
}

__global__ void __launch_bounds__(256) k_final(Ptrs p) {
  __shared__ float red[4][64];
  const int b = blockIdx.x / 7, cb = blockIdx.x % 7;
  const int tid = threadIdx.x, lane = tid & 63, wave = tid >> 6;
  const int br = wave >> 1;
  const float* y = (const float*)(p.ws + WS_Y) + (size_t)((br * 2 + b) * 64 + (wave & 1) * 32) * DM + cb * 64 + lane;
  float a = 0.f;
#pragma unroll
  for (int i = 0; i < 32; ++i) a += y[i * DM];
  red[wave][lane] = a;
  float gs = 0.f;
#pragma unroll
  for (int i = 0; i < 7; ++i) gs += bf_lo(p.x[b * DM + i * 64 + lane]) * bf_lo(p.gw[i * 64 + lane]);
#pragma unroll
  for (int o = 32; o; o >>= 1) gs += __shfl_xor(gs, o);
  __syncthreads();
  if (wave == 0) {
    float g = 1.f / (1.f + __expf(-gs));
    float A = red[0][lane] + red[1][lane], Bv = red[2][lane] + red[3][lane];
    p.out[b * DM + cb * 64 + lane] = f2bf(g * A + (1.f - g) * Bv);
  }
}

extern "C" void kernel_launch(void* const* d_in, const int* in_sizes, int n_in, void* d_out, int out_size, void* d_ws, size_t ws_size,
                              hipStream_t stream) {
  Ptrs p;
  p.x = (const unsigned short*)d_in[0];
  p.k[0] = (const unsigned short*)d_in[1]; p.v[0] = (const unsigned short*)d_in[2];
  p.wq[0] = (const unsigned short*)d_in[3]; p.wo[0] = (const unsigned short*)d_in[4];
  p.k[1] = (const unsigned short*)d_in[6]; p.v[1] = (const unsigned short*)d_in[7];
  p.wq[1] = (const unsigned short*)d_in[8]; p.wo[1] = (const unsigned short*)d_in[9];
  p.gw = (const unsigned short*)d_in[10];
  p.out = (unsigned short*)d_out; p.ws = (unsigned char*)d_ws;
  k_qproj<<<256, 512, 0, stream>>>(p);
  k_attn<<<256, 512, 0, stream>>>(p);
  k_comb<<<256, 512, 0, stream>>>(p);
  k_final<<<14, 256, 0, stream>>>(p);
}
```

```cpp
#include <hip/hip_runtime.h>
#include <hip/hip_bf16.h>
#include <stdint.h>

typedef unsigned u32x4 __attribute__((ext_vector_type(4)));
typedef unsigned u32x2 __attribute__((ext_vector_type(2)));
typedef float f32x4 __attribute__((ext_vector_type(4)));
typedef short bf16x8 __attribute__((ext_vector_type(8)));
typedef __bf16 bf16x2 __attribute__((ext_vector_type(2)));
typedef float f32x2 __attribute__((ext_vector_type(2)));
typedef unsigned long long u64;
typedef __attribute__((address_space(1))) u64 gu64;
typedef __attribute__((address_space(1))) unsigned gu32;
#define RLX_AGENT __ATOMIC_RELAXED, __HIP_MEMORY_SCOPE_AGENT

constexpr int DM = 448, HKV = 8, SK = 8192, DH = 256;
constexpr int NWAVE = 8, KEYS_WG = 1024;
#define DEPTH 4
constexpr int NCHUNK = 16;
constexpr unsigned SPIN_CAP = 1u << 18;

__device__ __forceinline__ float dot2(unsigned a, unsigned b, float acc) {
  return __builtin_amdgcn_fdot2_f32_bf16(__builtin_bit_cast(bf16x2, a), __builtin_bit_cast(bf16x2, b), acc, false);
}
template <int CTRL> __device__ __forceinline__ float dpp(float x) {
  return __builtin_bit_cast(float, __builtin_amdgcn_mov_dpp(__builtin_bit_cast(int, x), CTRL, 0xf, 0xf, true));
}
constexpr int XOR1 = 0xB1, XOR2 = 0x4E, XOR3 = 0x1B, XOR7 = 0x141, XOR8 = 0x128;
__device__ __forceinline__ float bf_lo(unsigned u) { return __builtin_bit_cast(float, u << 16); }
__device__ __forceinline__ float bf_hi(unsigned u) { return __builtin_bit_cast(float, u & 0xffff0000u); }
__device__ __forceinline__ unsigned pk_bf16(float lo, float hi) {
  bf16x2 v = __builtin_convertvector((f32x2){lo, hi}, bf16x2);
  return __builtin_bit_cast(unsigned, v);
}
__device__ __forceinline__ float rbf(float f) { return bf_lo(pk_bf16(f, f)); }
__device__ __forceinline__ float swap16_add(float a, float b) {
  auto r = __builtin_amdgcn_permlane16_swap(__builtin_bit_cast(unsigned, a), __builtin_bit_cast(unsigned, b), false, false);
  return __builtin_bit_cast(float, (unsigned)r[0]) + __builtin_bit_cast(float, (unsigned)r[1]);
}
__device__ __forceinline__ float swap32_add(float a, float b) {
  auto r = __builtin_amdgcn_permlane32_swap(__builtin_bit_cast(unsigned, a), __builtin_bit_cast(unsigned, b), false, false);
  return __builtin_bit_cast(float, (unsigned)r[0]) + __builtin_bit_cast(float, (unsigned)r[1]);
}
__device__ __forceinline__ float xrow_max(float a) {
  auto r = __builtin_amdgcn_permlane16_swap(__builtin_bit_cast(unsigned, a), __builtin_bit_cast(unsigned, a), false, false);
  a = fmaxf(__builtin_bit_cast(float, (unsigned)r[0]), __builtin_bit_cast(float, (unsigned)r[1]));
  auto s = __builtin_amdgcn_permlane32_swap(__builtin_bit_cast(unsigned, a), __builtin_bit_cast(unsigned, a), false, false);
  return fmaxf(__builtin_bit_cast(float, (unsigned)s[0]), __builtin_bit_cast(float, (unsigned)s[1]));
}
template <int CTRL> __device__ __forceinline__ float tr_step(float lo, float hi, bool sel) {
  float t1 = lo + dpp<CTRL>(lo), t2 = hi + dpp<CTRL>(hi);
  return sel ? t2 : t1;
}
__device__ __forceinline__ float xor4(float x) { return dpp<XOR3>(dpp<XOR7>(x)); }
#define WG_BARRIER() asm volatile("s_waitcnt lgkmcnt(0)\n\ts_barrier" ::: "memory")
__device__ __forceinline__ void put_granule(gu64* g, unsigned tag, unsigned v) { __hip_atomic_store(g, ((u64)tag << 32) | v, RLX_AGENT); }

struct Ptrs {
  const unsigned short* x;
  const unsigned short* k[2];
  const unsigned short* v[2];
  const unsigned short* wq[2];
  const unsigned short* wo[2];
  const unsigned short* gw;
  unsigned short* out;
  unsigned char* ws;
};
constexpr size_t WS_EPOCH = 0;
constexpr size_t WS_QG = 4096;
constexpr int OG_STRIDE = 512 + 16;
constexpr size_t WS_OG = WS_QG + 16 * 2 * 256 * 8;
constexpr size_t WS_YG = WS_OG + 256 * OG_STRIDE * 8;
#define STAMP(i)

__global__ void __launch_bounds__(512, 2) k_fused(Ptrs p) {
  __shared__ __attribute__((aligned(16))) unsigned q_l[256];
  __shared__ __attribute__((aligned(16))) float ol[NWAVE][512];
  __shared__ __attribute__((aligned(16))) float ml[NWAVE][4];
  __shared__ __attribute__((aligned(16))) float cv[NWAVE][64];
  __shared__ __attribute__((aligned(16))) float cml[NWAVE][2];
  __shared__ __attribute__((aligned(16))) float ow[NWAVE][64];
  __shared__ __attribute__((aligned(16))) float fin[NWAVE][4];
  __shared__ __attribute__((aligned(16))) unsigned misc[4];

  const int bid = blockIdx.x;
  const int combo = (bid & 7) * 2 + (bid >> 7), jq = (bid >> 3) & 15;
  const int br = combo >> 3, kvh = combo & 7, b = jq >> 3, s = jq & 7;
  const int tid = threadIdx.x, lane = tid & 63;
  const int wave = __builtin_amdgcn_readfirstlane(tid >> 6);
  STAMP(0);

  const size_t base = ((size_t)(b * HKV + kvh) * SK + s * KEYS_WG) * DH;
  const __amdgpu_buffer_rsrc_t rk = __builtin_amdgcn_make_buffer_rsrc((void*)(p.k[br] + base), 0, KEYS_WG * DH * 2, 0x00020000);
  const __amdgpu_buffer_rsrc_t rv = __builtin_amdgcn_make_buffer_rsrc((void*)(p.v[br] + base), 0, KEYS_WG * DH * 2, 0x00020000);
  const int voff = wave * 4096 + (lane >> 4) * 512 + (lane & 15) * 16;
  u32x4 ring[DEPTH][8];
  u32x4 wo[8];
#define LD(n, i) __builtin_amdgcn_raw_buffer_load_b128((i) < 4 ? rk : rv, voff + (n) * 32768, (((i) & 3) >> 1) * 2048 + ((i) & 1) * 256, 2)
#define RING_ISSUE() _Pragma("unroll") for (int d = 0; d < DEPTH; ++d) _Pragma("unroll") for (int i = 0; i < 8; ++i) ring[d][i] = LD(d, i)

  if (wave == 0) {
    const int u = lane >> 2, c4 = lane & 3;
    const unsigned epoch = __hip_atomic_load((gu32*)(p.ws + WS_EPOCH), RLX_AGENT);
    const unsigned short* wq = p.wq[br] + (size_t)(28 * u) * 4096 + kvh * 512 + jq * 32 + c4 * 8;
    u32x4 w[28];
#pragma unroll
    for (int i = 0; i < 28; ++i) w[i] = __builtin_nontemporal_load((const u32x4*)(wq + (size_t)i * 4096));
    unsigned xs[2][14], gws[14];
#pragma unroll
    for (int i = 0; i < 7; ++i) {
      u32x2 a = *(const u32x2*)(p.x + 28 * u + 4 * i), c = *(const u32x2*)(p.x + DM + 28 * u + 4 * i), e = *(const u32x2*)(p.gw + 28 * u + 4 * i);
      xs[0][2 * i] = a[0]; xs[0][2 * i + 1] = a[1]; xs[1][2 * i] = c[0]; xs[1][2 * i + 1] = c[1]; gws[2 * i] = e[0]; gws[2 * i + 1] = e[1];
    }
    const unsigned tag = __builtin_amdgcn_readfirstlane(epoch) + 1u;
    float a[16];
#pragma unroll
    for (int e = 0; e < 16; ++e) a[e] = 0.f;
#pragma unroll
    for (int i = 0; i < 14; ++i)
#pragma unroll
      for (int e = 0; e < 4; ++e) {
        const unsigned lo = __builtin_amdgcn_perm(w[2 * i + 1][e], w[2 * i][e], 0x05040100u);
        const unsigned hi = __builtin_amdgcn_perm(w[2 * i + 1][e], w[2 * i][e], 0x07060302u);
        a[2 * e] = dot2(lo, xs[0][i], a[2 * e]); a[2 * e + 1] = dot2(hi, xs[0][i], a[2 * e + 1]);
        a[8 + 2 * e] = dot2(lo, xs[1][i], a[8 + 2 * e]); a[8 + 2 * e + 1] = dot2(hi, xs[1][i], a[8 + 2 * e + 1]);
      }
#pragma unroll
    for (int e = 0; e < 8; ++e) a[e] = swap32_add(a[e], a[e + 8]);
#pragma unroll
    for (int e = 0; e < 4; ++e) a[e] = swap16_add(a[e], a[e + 4]);
#pragma unroll
    for (int e = 0; e < 2; ++e) a[e] = tr_step<XOR8>(a[e], a[e + 2], lane & 8);
    a[0] += xor4(a[0]); a[1] += xor4(a[1]);
    gu64* qg = (gu64*)(p.ws + WS_QG) + combo * 512;
    if ((lane & 4) == 0) put_granule(qg + (lane >> 5) * 256 + jq * 16 + c4 * 4 + ((lane >> 4) & 1) * 2 + ((lane >> 3) & 1), tag, pk_bf16(a[0], a[1]));
    STAMP(1);
    float g0 = 0.f, g1 = 0.f;
#pragma unroll
    for (int i = 0; i < 14; ++i) { g0 = dot2(xs[0][i], gws[i], g0); g1 = dot2(xs[1][i], gws[i], g1); }
    g0 = swap32_add(g0, g1); g0 = swap16_add(g0, g0); g0 += dpp<XOR8>(g0); g0 += xor4(g0);
    g0 = rbf(g0);
    g0 = rbf(1.f / (1.f + __expf(-g0)));
    if ((lane & 31) == 0) misc[1 + (lane >> 5)] = __builtin_bit_cast(unsigned, g0);
    if (lane == 0) misc[0] = tag;
    gu64* src = qg + b * 256 + lane;
    for (unsigned spin = 0;; ++spin) {
      bool ok = true;
      u64 v[4];
#pragma unroll
      for (int k = 0; k < 4; ++k) v[k] = __hip_atomic_load(src + 64 * k, RLX_AGENT);
#pragma unroll
      for (int k = 0; k < 4; ++k) { q_l[lane + 64 * k] = (unsigned)v[k]; ok &= (unsigned)(v[k] >> 32) == tag; }
      if (__all(ok) || spin > SPIN_CAP) break;
      __builtin_amdgcn_s_sleep(2);
    }
    STAMP(2);
    WG_BARRIER();
    RING_ISSUE();
#pragma unroll
    for (int i = 0; i < 8; ++i) wo[i] = u32x4{0u, 0u, 0u, 0u};
  } else {
    const unsigned short* wop = p.wo[br] + (size_t)(kvh * 512 + s * 64 + (lane >> 3) * 8) * DM + (wave - 1) * 64 + (lane & 7) * 8;
#pragma unroll
    for (int i = 0; i < 8; ++i) wo[i] = __builtin_nontemporal_load((const u32x4*)(wop + i * DM));
    RING_ISSUE();
    STAMP(1);
    WG_BARRIER();
    STAMP(2);
  }
  __builtin_amdgcn_sched_barrier(0);
  const unsigned tag = misc[0];
  u32x4 qa[2], qb[2];
  {
    const int h = (lane >> 3) & 1, c = lane & 15;
#pragma unroll
    for (int hf = 0; hf < 2; ++hf) {
      qa[hf] = *(const u32x4*)&q_l[h * 128 + hf * 64 + 4 * c];
      qb[hf] = *(const u32x4*)&q_l[(h ^ 1) * 128 + hf * 64 + 4 * c];
    }
  }
  unsigned msk[4];
#pragma unroll
  for (int r = 0; r < 4; ++r) msk[r] = (((lane & 7) >> 1) == r) ? ((lane & 1) ? 0xffff0000u : 0x0000ffffu) : 0u;
  constexpr float C2 = 0.0625f * 1.4426950408889634f;
  float m = -INFINITY, l = 0.f;
  f32x4 acc0 = {0.f, 0.f, 0.f, 0.f}, acc1 = {0.f, 0.f, 0.f, 0.f};
#pragma unroll 1
  for (int n = 0; n < NCHUNK; n += DEPTH) {
#pragma unroll
    for (int d = 0; d < DEPTH; ++d) {
      float sc[2];
#pragma unroll
      for (int j = 0; j < 2; ++j) {
        float xa = 0.f, xb = 0.f;
#pragma unroll
        for (int hf = 0; hf < 2; ++hf) {
          const u32x4 kk = ring[d][2 * j + hf];
#pragma unroll
          for (int e = 0; e < 4; ++e) { xa = dot2(kk[e], qa[hf][e], xa); xb = dot2(kk[e], qb[hf][e], xb); }
          asm volatile("" : "+v"(xa), "+v"(xb));
          ring[d][2 * j + hf] = LD(n + d + DEPTH, 2 * j + hf);
          __builtin_amdgcn_sched_barrier(0);
        }
        float y = xa + dpp<XOR8>(xb);
        y += dpp<XOR7>(y); y += dpp<XOR2>(y); y += dpp<XOR1>(y);
        sc[j] = y;
      }
      {
        const unsigned pk = pk_bf16(sc[0], sc[1]);
        sc[0] = bf_lo(pk); sc[1] = bf_hi(pk);
      }
      const float cm = fmaxf(sc[0], sc[1]);
      if (__any(cm > m)) {
        const float mn = fmaxf(m, xrow_max(cm));
        const float al = __builtin_amdgcn_exp2f((m - mn) * C2);
        const float alo = dpp<XOR8>(al);
        const float ad = (((lane >> 3) & 1) == (lane >> 5)) ? al : alo;
        l *= al; m = mn;
        acc0 *= ad; acc1 *= ad;
      }
      const float mc = m * C2;
#pragma unroll
      for (int j = 0; j < 2; ++j) {
        const float pv = __builtin_amdgcn_exp2f(fmaf(sc[j], C2, -mc));
        l += pv;
        const unsigned pd = pk_bf16(pv, pv);
        const u32x4 a = {pd & msk[0], pd & msk[1], pd & msk[2], pd & msk[3]};
        acc0 = __builtin_amdgcn_mfma_f32_16x16x32_bf16(__builtin_bit_cast(bf16x8, a), __builtin_bit_cast(bf16x8, ring[d][4 + 2 * j]), acc0, 0, 0, 0);
        asm volatile("" : "+v"(acc0));
        ring[d][4 + 2 * j] = LD(n + d + DEPTH, 4 + 2 * j);
        __builtin_amdgcn_sched_barrier(0);
        acc1 = __builtin_amdgcn_mfma_f32_16x16x32_bf16(__builtin_bit_cast(bf16x8, a), __builtin_bit_cast(bf16x8, ring[d][5 + 2 * j]), acc1, 0, 0, 0);
        asm volatile("" : "+v"(acc1));
        ring[d][5 + 2 * j] = LD(n + d + DEPTH, 5 + 2 * j);
        __builtin_amdgcn_sched_barrier(0);
      }
    }
  }
  STAMP(3);
  l = swap16_add(l, l); l = swap32_add(l, l);
  {
    const int rg = lane >> 4, h = rg >> 1, e0 = (rg & 1) * 4, c = lane & 15;
    *(f32x4*)&ol[wave][h * 256 + 8 * c + e0] = acc0;
    *(f32x4*)&ol[wave][h * 256 + 128 + 8 * c + e0] = acc1;
    if ((lane & 55) == 0) { f32x2 t = {m * C2, l}; *(f32x2*)&ml[wave][(lane >> 3) * 2] = t; }
  }
  WG_BARRIER();
  const int grp = (br * 2 + b) * 8 + kvh;
  {
    const int h = wave >> 2;
    f32x2 t[NWAVE];
#pragma unroll
    for (int w = 0; w < NWAVE; ++w) t[w] = *(const f32x2*)&ml[w][h * 2];
    float M = t[0][0];
#pragma unroll
    for (int w = 1; w < NWAVE; ++w) M = fmaxf(M, t[w][0]);
    float o = 0.f, L = 0.f;
#pragma unroll
    for (int w = 0; w < NWAVE; ++w) {
      const float sw = __builtin_amdgcn_exp2f(t[w][0] - M);
      o = fmaf(sw, ol[w][tid], o); L = fmaf(sw, t[w][1], L);
    }
    gu64* og = (gu64*)(p.ws + WS_OG) + (size_t)(grp * 8 + s) * OG_STRIDE;
    put_granule(og + tid, tag, __builtin_bit_cast(unsigned, o));
    if ((tid & 255) < 2) put_granule(og + 512 + h * 2 + (tid & 1), tag, __builtin_bit_cast(unsigned, (tid & 1) ? L : M));
  }
  STAMP(4);
  {
    gu64* src = (gu64*)(p.ws + WS_OG) + (size_t)(grp * 8 + wave) * OG_STRIDE;
    u64 v, t;
    for (unsigned spin = 0;; ++spin) {
      v = __hip_atomic_load(src + s * 64 + lane, RLX_AGENT);
      t = __hip_atomic_load(src + 512 + (s >> 2) * 2 + (lane & 1), RLX_AGENT);
      const bool ok = (unsigned)(v >> 32) == tag && (unsigned)(t >> 32) == tag;
      if (__all(ok) || spin > SPIN_CAP) break;
      __builtin_amdgcn_s_sleep(4);
    }
    cv[wave][lane] = __builtin_bit_cast(float, (unsigned)v);
    if (lane < 2) cml[wave][lane] = __builtin_bit_cast(float, (unsigned)t);
  }
  STAMP(5);
  WG_BARRIER();
  if (wave > 0) {
    f32x2 t[NWAVE];
#pragma unroll
    for (int w = 0; w < NWAVE; ++w) t[w] = *(const f32x2*)&cml[w][0];
    float M = t[0][0];
#pragma unroll
    for (int w = 1; w < NWAVE; ++w) M = fmaxf(M, t[w][0]);
    float o = 0.f, L = 0.f;
#pragma unroll
    for (int w = 0; w < NWAVE; ++w) {
      const float sw = __builtin_amdgcn_exp2f(t[w][0] - M);
      o = fmaf(sw, cv[w][lane], o); L = fmaf(sw, t[w][1], L);
    }
    ow[wave][lane] = rbf(o / L);
    asm volatile("s_waitcnt lgkmcnt(0)" ::: "memory");
    const f32x4 o0 = *(const f32x4*)&ow[wave][(lane >> 3) * 8], o1 = *(const f32x4*)&ow[wave][(lane >> 3) * 8 + 4];
    float a[8];
#pragma unroll
    for (int e = 0; e < 8; ++e) a[e] = 0.f;
#pragma unroll
    for (int i = 0; i < 8; ++i) {
      const float ov = i < 4 ? o0[i & 3] : o1[i & 3];
#pragma unroll
      for (int e = 0; e < 4; ++e) { a[2 * e] = fmaf(ov, bf_lo(wo[i][e]), a[2 * e]); a[2 * e + 1] = fmaf(ov, bf_hi(wo[i][e]), a[2 * e + 1]); }
    }
#pragma unroll
    for (int e = 0; e < 4; ++e) a[e] = swap32_add(a[e], a[e + 4]);
#pragma unroll
    for (int e = 0; e < 2; ++e) a[e] = swap16_add(a[e], a[e + 2]);
    a[0] = tr_step<XOR8>(a[0], a[1], lane & 8);
    const int e = ((lane >> 5) & 1) * 4 + ((lane >> 4) & 1) * 2 + ((lane >> 3) & 1);
    put_granule((gu64*)(p.ws + WS_YG) + (size_t)(grp * 8 + s) * DM + (wave - 1) * 64 + (lane & 7) * 8 + e, tag, __builtin_bit_cast(unsigned, a[0]));
  }
  STAMP(6);
  if (bid < 224) {
    const int fb = bid >= 112 ? 1 : 0, cb = bid - fb * 112;
    const int prod = tid >> 2;
    gu64* src = (gu64*)(p.ws + WS_YG) + (size_t)(((prod >> 6) * 2 + fb) * 64 + (prod & 63)) * DM + cb * 4 + (tid & 3);
    u64 v;
    for (unsigned spin = 0;; ++spin) {
      v = __hip_atomic_load(src, RLX_AGENT);
      if (__all((unsigned)(v >> 32) == tag) || spin > SPIN_CAP) break;
      __builtin_amdgcn_s_sleep(4);
    }
    float y = __builtin_bit_cast(float, (unsigned)v);
    y = swap32_add(y, y); y = swap16_add(y, y); y += dpp<XOR8>(y); y += xor4(y);
    if (lane < 4) fin[wave][lane] = y;
    STAMP(7);
    WG_BARRIER();
    if (tid < 4) {
      const float A = rbf((fin[0][tid] + fin[1][tid]) + (fin[2][tid] + fin[3][tid]));
      const float B = rbf((fin[4][tid] + fin[5][tid]) + (fin[6][tid] + fin[7][tid]));
      const float g = __builtin_bit_cast(float, misc[1 + fb]);
      const float r = rbf(g * A) + rbf(rbf(1.f - g) * B);
      p.out[fb * DM + cb * 4 + tid] = (unsigned short)pk_bf16(r, r);
      if (bid == 0 && tid == 0) __hip_atomic_store((gu32*)(p.ws + WS_EPOCH), tag, RLX_AGENT);
    }
    STAMP(8);
  }
}

extern "C" void kernel_launch(void* const* d_in, const int* in_sizes, int n_in, void* d_out, int out_size, void* d_ws, size_t ws_size,
                              hipStream_t stream) {
  Ptrs p;
  p.x = (const unsigned short*)d_in[0];
  p.k[0] = (const unsigned short*)d_in[1]; p.v[0] = (const unsigned short*)d_in[2];
  p.wq[0] = (const unsigned short*)d_in[3]; p.wo[0] = (const unsigned short*)d_in[4];
  p.k[1] = (const unsigned short*)d_in[6]; p.v[1] = (const unsigned short*)d_in[7];
  p.wq[1] = (const unsigned short*)d_in[8]; p.wo[1] = (const unsigned short*)d_in[9];
  p.gw = (const unsigned short*)d_in[10];
  p.out = (unsigned short*)d_out; p.ws = (unsigned char*)d_ws;
  k_fused<<<256, 512, 0, stream>>>(p);
}
```

```cpp
#define STAMPS
#include <stdio.h>
#include <hip/hip_runtime.h>
#include <hip/hip_bf16.h>
#include <stdint.h>

typedef unsigned u32x4 __attribute__((ext_vector_type(4)));
typedef unsigned u32x2 __attribute__((ext_vector_type(2)));
typedef float f32x4 __attribute__((ext_vector_type(4)));
typedef short bf16x8 __attribute__((ext_vector_type(8)));
typedef __bf16 bf16x2 __attribute__((ext_vector_type(2)));
typedef float f32x2 __attribute__((ext_vector_type(2)));
typedef unsigned long long u64;
typedef __attribute__((address_space(1))) u64 gu64;
typedef __attribute__((address_space(1))) unsigned gu32;
#define RLX_AGENT __ATOMIC_RELAXED, __HIP_MEMORY_SCOPE_AGENT

constexpr int DM = 448, HKV = 8, SK = 8192, DH = 256;
constexpr int NWAVE = 8, KEYS_WG = 1024;
#define DEPTH 4
constexpr int NCHUNK = 16;
constexpr unsigned SPIN_CAP = 1u << 18;

__device__ __forceinline__ float dot2(unsigned a, unsigned b, float acc) {
  return __builtin_amdgcn_fdot2_f32_bf16(__builtin_bit_cast(bf16x2, a), __builtin_bit_cast(bf16x2, b), acc, false);
}
template <int CTRL> __device__ __forceinline__ float dpp(float x) {
  return __builtin_bit_cast(float, __builtin_amdgcn_mov_dpp(__builtin_bit_cast(int, x), CTRL, 0xf, 0xf, true));
}
constexpr int XOR1 = 0xB1, XOR2 = 0x4E, XOR3 = 0x1B, XOR7 = 0x141, XOR8 = 0x128;
__device__ __forceinline__ float bf_lo(unsigned u) { return __builtin_bit_cast(float, u << 16); }
__device__ __forceinline__ float bf_hi(unsigned u) { return __builtin_bit_cast(float, u & 0xffff0000u); }
__device__ __forceinline__ unsigned pk_bf16(float lo, float hi) {
  bf16x2 v = __builtin_convertvector((f32x2){lo, hi}, bf16x2);
  return __builtin_bit_cast(unsigned, v);
}
__device__ __forceinline__ float rbf(float f) { return bf_lo(pk_bf16(f, f)); }
__device__ __forceinline__ float swap16_add(float a, float b) {
  auto r = __builtin_amdgcn_permlane16_swap(__builtin_bit_cast(unsigned, a), __builtin_bit_cast(unsigned, b), false, false);
  return __builtin_bit_cast(float, (unsigned)r[0]) + __builtin_bit_cast(float, (unsigned)r[1]);
}
__device__ __forceinline__ float swap32_add(float a, float b) {
  auto r = __builtin_amdgcn_permlane32_swap(__builtin_bit_cast(unsigned, a), __builtin_bit_cast(unsigned, b), false, false);
  return __builtin_bit_cast(float, (unsigned)r[0]) + __builtin_bit_cast(float, (unsigned)r[1]);
}
__device__ __forceinline__ float xrow_max(float a) {
  auto r = __builtin_amdgcn_permlane16_swap(__builtin_bit_cast(unsigned, a), __builtin_bit_cast(unsigned, a), false, false);
  a = fmaxf(__builtin_bit_cast(float, (unsigned)r[0]), __builtin_bit_cast(float, (unsigned)r[1]));
  auto s = __builtin_amdgcn_permlane32_swap(__builtin_bit_cast(unsigned, a), __builtin_bit_cast(unsigned, a), false, false);
  return fmaxf(__builtin_bit_cast(float, (unsigned)s[0]), __builtin_bit_cast(float, (unsigned)s[1]));
}
template <int CTRL> __device__ __forceinline__ float tr_step(float lo, float hi, bool sel) {
  float t1 = lo + dpp<CTRL>(lo), t2 = hi + dpp<CTRL>(hi);
  return sel ? t2 : t1;
}
__device__ __forceinline__ float xor4(float x) { return dpp<XOR3>(dpp<XOR7>(x)); }
#define WG_BARRIER() asm volatile("s_waitcnt lgkmcnt(0)\n\ts_barrier" ::: "memory")
__device__ __forceinline__ void put_granule(gu64* g, unsigned tag, unsigned v) { __hip_atomic_store(g, ((u64)tag << 32) | v, RLX_AGENT); }

struct Ptrs {
  const unsigned short* x;
  const unsigned short* k[2];
  const unsigned short* v[2];
  const unsigned short* wq[2];
  const unsigned short* wo[2];
  const unsigned short* gw;
  unsigned short* out;
  unsigned char* ws;
};
constexpr size_t WS_EPOCH = 0;
constexpr size_t WS_QG = 4096;
constexpr int OG_STRIDE = 512 + 16;
constexpr size_t WS_OG = WS_QG + 16 * 2 * 256 * 8;
constexpr size_t WS_YG = WS_OG + 256 * OG_STRIDE * 8;
constexpr size_t WS_ST = 8u << 20;
#define STAMP(i) do { if (lane == 0) ((u64*)(p.ws + WS_ST))[(blockIdx.x * 8 + wave) * 16 + (i)] = __builtin_amdgcn_s_memrealtime(); } while (0)

__global__ void __launch_bounds__(512, 2) k_fused(Ptrs p) {
  __shared__ __attribute__((aligned(16))) unsigned q_l[256];
  __shared__ __attribute__((aligned(16))) float ol[NWAVE][512];
  __shared__ __attribute__((aligned(16))) float ml[NWAVE][4];
  __shared__ __attribute__((aligned(16))) float cv[NWAVE][64];
  __shared__ __attribute__((aligned(16))) float cml[NWAVE][2];
  __shared__ __attribute__((aligned(16))) float ow[NWAVE][64];
  __shared__ __attribute__((aligned(16))) float fin[NWAVE][4];
  __shared__ __attribute__((aligned(16))) unsigned misc[4];

  const int bid = blockIdx.x;
  const int combo = (bid & 7) * 2 + (bid >> 7), jq = (bid >> 3) & 15;
  const int br = combo >> 3, kvh = combo & 7, b = jq >> 3, s = jq & 7;
  const int tid = threadIdx.x, lane = tid & 63;
  const int wave = __builtin_amdgcn_readfirstlane(tid >> 6);
  STAMP(0);

  const size_t base = ((size_t)(b * HKV + kvh) * SK + s * KEYS_WG) * DH;
  const __amdgpu_buffer_rsrc_t rk = __builtin_amdgcn_make_buffer_rsrc((void*)(p.k[br] + base), 0, KEYS_WG * DH * 2, 0x00020000);
  const __amdgpu_buffer_rsrc_t rv = __builtin_amdgcn_make_buffer_rsrc((void*)(p.v[br] + base), 0, KEYS_WG * DH * 2, 0x00020000);
  const int voff = wave * 4096 + (lane >> 4) * 512 + (lane & 15) * 16;
  u32x4 ring[DEPTH][8];
  u32x4 wo[8];
#define LD(n, i) __builtin_amdgcn_raw_buffer_load_b128((i) < 4 ? rk : rv, voff + (n) * 32768, (((i) & 3) >> 1) * 2048 + ((i) & 1) * 256, 2)
#define RING_ISSUE() _Pragma("unroll") for (int d = 0; d < DEPTH; ++d) _Pragma("unroll") for (int i = 0; i < 8; ++i) ring[d][i] = LD(d, i)

  if (wave == 0) {
    const int u = lane >> 2, c4 = lane & 3;
    const unsigned epoch = __hip_atomic_load((gu32*)(p.ws + WS_EPOCH), RLX_AGENT);
    const unsigned short* wq = p.wq[br] + (size_t)(28 * u) * 4096 + kvh * 512 + jq * 32 + c4 * 8;
    u32x4 w[28];
#pragma unroll
    for (int i = 0; i < 28; ++i) w[i] = __builtin_nontemporal_load((const u32x4*)(wq + (size_t)i * 4096));
    unsigned xs[2][14], gws[14];
#pragma unroll
    for (int i = 0; i < 7; ++i) {
      u32x2 a = *(const u32x2*)(p.x + 28 * u + 4 * i), c = *(const u32x2*)(p.x + DM + 28 * u + 4 * i), e = *(const u32x2*)(p.gw + 28 * u + 4 * i);
      xs[0][2 * i] = a[0]; xs[0][2 * i + 1] = a[1]; xs[1][2 * i] = c[0]; xs[1][2 * i + 1] = c[1]; gws[2 * i] = e[0]; gws[2 * i + 1] = e[1];
    }
    const unsigned tag = __builtin_amdgcn_readfirstlane(epoch) + 1u;
    float a[16];
#pragma unroll
    for (int e = 0; e < 16; ++e) a[e] = 0.f;
#pragma unroll
    for (int i = 0; i < 14; ++i)
#pragma unroll
      for (int e = 0; e < 4; ++e) {
        const unsigned lo = __builtin_amdgcn_perm(w[2 * i + 1][e], w[2 * i][e], 0x05040100u);
        const unsigned hi = __builtin_amdgcn_perm(w[2 * i + 1][e], w[2 * i][e], 0x07060302u);
        a[2 * e] = dot2(lo, xs[0][i], a[2 * e]); a[2 * e + 1] = dot2(hi, xs[0][i], a[2 * e + 1]);
        a[8 + 2 * e] = dot2(lo, xs[1][i], a[8 + 2 * e]); a[8 + 2 * e + 1] = dot2(hi, xs[1][i], a[8 + 2 * e + 1]);
      }
#pragma unroll
    for (int e = 0; e < 8; ++e) a[e] = swap32_add(a[e], a[e + 8]);
#pragma unroll
    for (int e = 0; e < 4; ++e) a[e] = swap16_add(a[e], a[e + 4]);
#pragma unroll
    for (int e = 0; e < 2; ++e) a[e] = tr_step<XOR8>(a[e], a[e + 2], lane & 8);
    a[0] += xor4(a[0]); a[1] += xor4(a[1]);
    gu64* qg = (gu64*)(p.ws + WS_QG) + combo * 512;
    if ((lane & 4) == 0) put_granule(qg + (lane >> 5) * 256 + jq * 16 + c4 * 4 + ((lane >> 4) & 1) * 2 + ((lane >> 3) & 1), tag, pk_bf16(a[0], a[1]));
    STAMP(1);
    float g0 = 0.f, g1 = 0.f;
#pragma unroll
    for (int i = 0; i < 14; ++i) { g0 = dot2(xs[0][i], gws[i], g0); g1 = dot2(xs[1][i], gws[i], g1); }
    g0 = swap32_add(g0, g1); g0 = swap16_add(g0, g0); g0 += dpp<XOR8>(g0); g0 += xor4(g0);
    g0 = rbf(g0);
    g0 = rbf(1.f / (1.f + __expf(-g0)));
    if ((lane & 31) == 0) misc[1 + (lane >> 5)] = __builtin_bit_cast(unsigned, g0);
    if (lane == 0) misc[0] = tag;
    gu64* src = qg + b * 256 + lane;
    for (unsigned spin = 0;; ++spin) {
      bool ok = true;
      u64 v[4];
#pragma unroll
      for (int k = 0; k < 4; ++k) v[k] = __hip_atomic_load(src + 64 * k, RLX_AGENT);
#pragma unroll
      for (int k = 0; k < 4; ++k) { q_l[lane + 64 * k] = (unsigned)v[k]; ok &= (unsigned)(v[k] >> 32) == tag; }
      if (__all(ok) || spin > SPIN_CAP) break;
      __builtin_amdgcn_s_sleep(2);
    }
    STAMP(2);
    WG_BARRIER();
    RING_ISSUE();
#pragma unroll
    for (int i = 0; i < 8; ++i) wo[i] = u32x4{0u, 0u, 0u, 0u};
  } else {
    const unsigned short* wop = p.wo[br] + (size_t)(kvh * 512 + s * 64 + (lane >> 3) * 8) * DM + (wave - 1) * 64 + (lane & 7) * 8;
#pragma unroll
    for (int i = 0; i < 8; ++i) wo[i] = __builtin_nontemporal_load((const u32x4*)(wop + i * DM));
    RING_ISSUE();
    STAMP(1);
    WG_BARRIER();
    STAMP(2);
  }
  __builtin_amdgcn_sched_barrier(0);
  const unsigned tag = misc[0];
  u32x4 qa[2], qb[2];
  {
    const int h = (lane >> 3) & 1, c = lane & 15;
#pragma unroll
    for (int hf = 0; hf < 2; ++hf) {
      qa[hf] = *(const u32x4*)&q_l[h * 128 + hf * 64 + 4 * c];
      qb[hf] = *(const u32x4*)&q_l[(h ^ 1) * 128 + hf * 64 + 4 * c];
    }
  }
  unsigned msk[4];
#pragma unroll
  for (int r = 0; r < 4; ++r) msk[r] = (((lane & 7) >> 1) == r) ? ((lane & 1) ? 0xffff0000u : 0x0000ffffu) : 0u;
  constexpr float C2 = 0.0625f * 1.4426950408889634f;
  float m = -INFINITY, l = 0.f;
  f32x4 acc0 = {0.f, 0.f, 0.f, 0.f}, acc1 = {0.f, 0.f, 0.f, 0.f};
#pragma unroll 1
  for (int n = 0; n < NCHUNK; n += DEPTH) {
#pragma unroll
    for (int d = 0; d < DEPTH; ++d) {
      float sc[2];
#pragma unroll
      for (int j = 0; j < 2; ++j) {
        float xa = 0.f, xb = 0.f;
#pragma unroll
        for (int hf = 0; hf < 2; ++hf) {
          const u32x4 kk = ring[d][2 * j + hf];
#pragma unroll
          for (int e = 0; e < 4; ++e) { xa = dot2(kk[e], qa[hf][e], xa); xb = dot2(kk[e], qb[hf][e], xb); }
          asm volatile("" : "+v"(xa), "+v"(xb));
          ring[d][2 * j + hf] = LD(n + d + DEPTH, 2 * j + hf);
          __builtin_amdgcn_sched_barrier(0);
        }
        float y = xa + dpp<XOR8>(xb);
        y += dpp<XOR7>(y); y += dpp<XOR2>(y); y += dpp<XOR1>(y);
        sc[j] = y;
      }
      {
        const unsigned pk = pk_bf16(sc[0], sc[1]);
        sc[0] = bf_lo(pk); sc[1] = bf_hi(pk);
      }
      const float cm = fmaxf(sc[0], sc[1]);
      if (__any(cm > m)) {
        const float mn = fmaxf(m, xrow_max(cm));
        const float al = __builtin_amdgcn_exp2f((m - mn) * C2);
        const float alo = dpp<XOR8>(al);
        const float ad = (((lane >> 3) & 1) == (lane >> 5)) ? al : alo;
        l *= al; m = mn;
        acc0 *= ad; acc1 *= ad;
      }
      const float mc = m * C2;
#pragma unroll
      for (int j = 0; j < 2; ++j) {
        const float pv = __builtin_amdgcn_exp2f(fmaf(sc[j], C2, -mc));
        l += pv;
        const unsigned pd = pk_bf16(pv, pv);
        const u32x4 a = {pd & msk[0], pd & msk[1], pd & msk[2], pd & msk[3]};
        acc0 = __builtin_amdgcn_mfma_f32_16x16x32_bf16(__builtin_bit_cast(bf16x8, a), __builtin_bit_cast(bf16x8, ring[d][4 + 2 * j]), acc0, 0, 0, 0);
        asm volatile("" : "+v"(acc0));
        ring[d][4 + 2 * j] = LD(n + d + DEPTH, 4 + 2 * j);
        __builtin_amdgcn_sched_barrier(0);
        acc1 = __builtin_amdgcn_mfma_f32_16x16x32_bf16(__builtin_bit_cast(bf16x8, a), __builtin_bit_cast(bf16x8, ring[d][5 + 2 * j]), acc1, 0, 0, 0);
        asm volatile("" : "+v"(acc1));
        ring[d][5 + 2 * j] = LD(n + d + DEPTH, 5 + 2 * j);
        __builtin_amdgcn_sched_barrier(0);
      }
    }
  }
  STAMP(3);
  l = swap16_add(l, l); l = swap32_add(l, l);
  {
    const int rg = lane >> 4, h = rg >> 1, e0 = (rg & 1) * 4, c = lane & 15;
    *(f32x4*)&ol[wave][h * 256 + 8 * c + e0] = acc0;
    *(f32x4*)&ol[wave][h * 256 + 128 + 8 * c + e0] = acc1;
    if ((lane & 55) == 0) { f32x2 t = {m * C2, l}; *(f32x2*)&ml[wave][(lane >> 3) * 2] = t; }
  }
  WG_BARRIER();
  const int grp = (br * 2 + b) * 8 + kvh;
  {
    const int h = wave >> 2;
    f32x2 t[NWAVE];
#pragma unroll
    for (int w = 0; w < NWAVE; ++w) t[w] = *(const f32x2*)&ml[w][h * 2];
    float M = t[0][0];
#pragma unroll
    for (int w = 1; w < NWAVE; ++w) M = fmaxf(M, t[w][0]);
    float o = 0.f, L = 0.f;
#pragma unroll
    for (int w = 0; w < NWAVE; ++w) {
      const float sw = __builtin_amdgcn_exp2f(t[w][0] - M);
      o = fmaf(sw, ol[w][tid], o); L = fmaf(sw, t[w][1], L);
    }
    gu64* og = (gu64*)(p.ws + WS_OG) + (size_t)(grp * 8 + s) * OG_STRIDE;
    put_granule(og + tid, tag, __builtin_bit_cast(unsigned, o));
    if ((tid & 255) < 2) put_granule(og + 512 + h * 2 + (tid & 1), tag, __builtin_bit_cast(unsigned, (tid & 1) ? L : M));
  }
  STAMP(4);
  {
    gu64* src = (gu64*)(p.ws + WS_OG) + (size_t)(grp * 8 + wave) * OG_STRIDE;
    u64 v, t;
    for (unsigned spin = 0;; ++spin) {
      v = __hip_atomic_load(src + s * 64 + lane, RLX_AGENT);
      t = __hip_atomic_load(src + 512 + (s >> 2) * 2 + (lane & 1), RLX_AGENT);
      const bool ok = (unsigned)(v >> 32) == tag && (unsigned)(t >> 32) == tag;
      if (__all(ok) || spin > SPIN_CAP) break;
      __builtin_amdgcn_s_sleep(4);
    }
    cv[wave][lane] = __builtin_bit_cast(float, (unsigned)v);
    if (lane < 2) cml[wave][lane] = __builtin_bit_cast(float, (unsigned)t);
  }
  STAMP(5);
  WG_BARRIER();
  if (wave > 0) {
    f32x2 t[NWAVE];
#pragma unroll
    for (int w = 0; w < NWAVE; ++w) t[w] = *(const f32x2*)&cml[w][0];
    float M = t[0][0];
#pragma unroll
    for (int w = 1; w < NWAVE; ++w) M = fmaxf(M, t[w][0]);
    float o = 0.f, L = 0.f;
#pragma unroll
    for (int w = 0; w < NWAVE; ++w) {
      const float sw = __builtin_amdgcn_exp2f(t[w][0] - M);
      o = fmaf(sw, cv[w][lane], o); L = fmaf(sw, t[w][1], L);
    }
    ow[wave][lane] = rbf(o / L);
    asm volatile("s_waitcnt lgkmcnt(0)" ::: "memory");
    const f32x4 o0 = *(const f32x4*)&ow[wave][(lane >> 3) * 8], o1 = *(const f32x4*)&ow[wave][(lane >> 3) * 8 + 4];
    float a[8];
#pragma unroll
    for (int e = 0; e < 8; ++e) a[e] = 0.f;
#pragma unroll
    for (int i = 0; i < 8; ++i) {
      const float ov = i < 4 ? o0[i & 3] : o1[i & 3];
#pragma unroll
      for (int e = 0; e < 4; ++e) { a[2 * e] = fmaf(ov, bf_lo(wo[i][e]), a[2 * e]); a[2 * e + 1] = fmaf(ov, bf_hi(wo[i][e]), a[2 * e + 1]); }
    }
#pragma unroll
    for (int e = 0; e < 4; ++e) a[e] = swap32_add(a[e], a[e + 4]);
#pragma unroll
    for (int e = 0; e < 2; ++e) a[e] = swap16_add(a[e], a[e + 2]);
    a[0] = tr_step<XOR8>(a[0], a[1], lane & 8);
    const int e = ((lane >> 5) & 1) * 4 + ((lane >> 4) & 1) * 2 + ((lane >> 3) & 1);
    put_granule((gu64*)(p.ws + WS_YG) + (size_t)(grp * 8 + s) * DM + (wave - 1) * 64 + (lane & 7) * 8 + e, tag, __builtin_bit_cast(unsigned, a[0]));
  }
  STAMP(6);
  if (bid < 224) {
    const int fb = bid >= 112 ? 1 : 0, cb = bid - fb * 112;
    const int prod = tid >> 2;
    gu64* src = (gu64*)(p.ws + WS_YG) + (size_t)(((prod >> 6) * 2 + fb) * 64 + (prod & 63)) * DM + cb * 4 + (tid & 3);
    u64 v;
    for (unsigned spin = 0;; ++spin) {
      v = __hip_atomic_load(src, RLX_AGENT);
      if (__all((unsigned)(v >> 32) == tag) || spin > SPIN_CAP) break;
      __builtin_amdgcn_s_sleep(4);
    }
    float y = __builtin_bit_cast(float, (unsigned)v);
    y = swap32_add(y, y); y = swap16_add(y, y); y += dpp<XOR8>(y); y += xor4(y);
    if (lane < 4) fin[wave][lane] = y;
    STAMP(7);
    WG_BARRIER();
    if (tid < 4) {
      const float A = rbf((fin[0][tid] + fin[1][tid]) + (fin[2][tid] + fin[3][tid]));
      const float B = rbf((fin[4][tid] + fin[5][tid]) + (fin[6][tid] + fin[7][tid]));
      const float g = __builtin_bit_cast(float, misc[1 + fb]);
      const float r = rbf(g * A) + rbf(rbf(1.f - g) * B);
      p.out[fb * DM + cb * 4 + tid] = (unsigned short)pk_bf16(r, r);
      if (bid == 0 && tid == 0) __hip_atomic_store((gu32*)(p.ws + WS_EPOCH), tag, RLX_AGENT);
    }
    STAMP(8);
  }
}

__global__ void k_report(const u64* s) {
  if (threadIdx.x != 0) return;
  u64 t0 = ~0ull;
  for (int w = 0; w < 2048; ++w) t0 = s[w * 16] < t0 ? s[w * 16] : t0;
  for (int k = 0; k < 9; ++k) {
    for (int g = 0; g < 3; ++g) {
      u64 mn = ~0ull, mx = 0, sum = 0; int c = 0;
      for (int w = 0; w < (k >= 7 ? 224 * 8 : 2048); ++w) { int wv = w & 7; int gg = wv == 0 ? 0 : wv < 4 ? 1 : 2; if (gg != g) continue; u64 v = s[w * 16 + k] - t0; mn = v < mn ? v : mn; mx = v > mx ? v : mx; sum += v; ++c; }
      printf(" st%d g%d: min %d avg %d max %d |", k, g, (int)mn, (int)(sum / c), (int)mx);
    }
    printf("\n");
  }
}
extern "C" void kernel_launch(void* const* d_in, const int* in_sizes, int n_in, void* d_out, int out_size, void* d_ws, size_t ws_size,
                              hipStream_t stream) {
  Ptrs p;
  p.x = (const unsigned short*)d_in[0];
  p.k[0] = (const unsigned short*)d_in[1]; p.v[0] = (const unsigned short*)d_in[2];
  p.wq[0] = (const unsigned short*)d_in[3]; p.wo[0] = (const unsigned short*)d_in[4];
  p.k[1] = (const unsigned short*)d_in[6]; p.v[1] = (const unsigned short*)d_in[7];
  p.wq[1] = (const unsigned short*)d_in[8]; p.wo[1] = (const unsigned short*)d_in[9];
  p.gw = (const unsigned short*)d_in[10];
  p.out = (unsigned short*)d_out; p.ws = (unsigned char*)d_ws;
  for (int i = 0; i < 11; ++i) fprintf(stderr, "in%d %p\n", i, d_in[i]);
  fprintf(stderr, "out %p ws %p\n", d_out, d_ws);
  k_fused<<<256, 512, 0, stream>>>(p);
  k_report<<<1, 64, 0, stream>>>((const u64*)(p.ws + WS_ST));
}
```

```cpp
#include <hip/hip_runtime.h>
#include <hip/hip_bf16.h>
#include <stdint.h>
#include <utility>

typedef unsigned u32x4 __attribute__((ext_vector_type(4)));
typedef unsigned u32x2 __attribute__((ext_vector_type(2)));
typedef float f32x4 __attribute__((ext_vector_type(4)));
typedef float f32x2 __attribute__((ext_vector_type(2)));
typedef short bf16x8 __attribute__((ext_vector_type(8)));
typedef __bf16 bf16x2 __attribute__((ext_vector_type(2)));
typedef unsigned long long u64;
typedef __attribute__((address_space(1))) unsigned gu32;
typedef __attribute__((address_space(1))) const u32x4 gcu32x4;
#define RLX_AGENT __ATOMIC_RELAXED, __HIP_MEMORY_SCOPE_AGENT
#define RLX_WG __ATOMIC_RELAXED, __HIP_MEMORY_SCOPE_WORKGROUP

constexpr int DM = 448, HKV = 8, SK = 8192, DH = 256, NWAVE = 8;
constexpr int NCH = 1024;
constexpr int SB = 6;
constexpr int NS = 64 + 8 * SB;
constexpr int DBASE = 128 * SB;
constexpr int NP = 2;
constexpr int DYNW = 4;
constexpr int PRE = 20;
constexpr int CAP = 3, PCAP = 4, PCUNTIL = 32;
constexpr int PACE = 436, POFF = 200;
constexpr unsigned SPIN_CAP = 1u << 18;

template <int... I, class F> __device__ __forceinline__ void static_for_impl(std::integer_sequence<int, I...>, F&& f) { (f(std::integral_constant<int, I>{}), ...); }
template <int N, class F> __device__ __forceinline__ void static_for(F&& f) { static_for_impl(std::make_integer_sequence<int, N>{}, f); }
template <int N> using IC = std::integral_constant<int, N>;

__device__ __forceinline__ float dot2(unsigned a, unsigned b, float acc) {
  return __builtin_amdgcn_fdot2_f32_bf16(__builtin_bit_cast(bf16x2, a), __builtin_bit_cast(bf16x2, b), acc, false);
}
__device__ __forceinline__ f32x4 mfma(u32x4 a, u32x4 b, f32x4 c) {
  return __builtin_amdgcn_mfma_f32_16x16x32_bf16(__builtin_bit_cast(bf16x8, a), __builtin_bit_cast(bf16x8, b), c, 0, 0, 0);
}
template <int CTRL> __device__ __forceinline__ float dpp(float x) {
  return __builtin_bit_cast(float, __builtin_amdgcn_mov_dpp(__builtin_bit_cast(int, x), CTRL, 0xf, 0xf, true));
}
constexpr int XOR1 = 0xB1, XOR2 = 0x4E, XOR3 = 0x1B, XOR7 = 0x141, XOR8 = 0x128, BCAST = 0x150;
__device__ __forceinline__ float bf_lo(unsigned u) { return __builtin_bit_cast(float, u << 16); }
__device__ __forceinline__ float bf_hi(unsigned u) { return __builtin_bit_cast(float, u & 0xffff0000u); }
__device__ __forceinline__ unsigned pk_bf16(float lo, float hi) {
  return __builtin_bit_cast(unsigned, __builtin_convertvector((f32x2){lo, hi}, bf16x2));
}
__device__ __forceinline__ float rbf(float f) { return bf_lo(pk_bf16(f, f)); }
__device__ __forceinline__ float swap16_add(float a, float b) {
  auto r = __builtin_amdgcn_permlane16_swap(__builtin_bit_cast(unsigned, a), __builtin_bit_cast(unsigned, b), false, false);
  return __builtin_bit_cast(float, (unsigned)r[0]) + __builtin_bit_cast(float, (unsigned)r[1]);
}
__device__ __forceinline__ float swap32_add(float a, float b) {
  auto r = __builtin_amdgcn_permlane32_swap(__builtin_bit_cast(unsigned, a), __builtin_bit_cast(unsigned, b), false, false);
  return __builtin_bit_cast(float, (unsigned)r[0]) + __builtin_bit_cast(float, (unsigned)r[1]);
}
__device__ __forceinline__ float xrow_max(float a) {
  auto r = __builtin_amdgcn_permlane16_swap(__builtin_bit_cast(unsigned, a), __builtin_bit_cast(unsigned, a), false, false);
  a = fmaxf(__builtin_bit_cast(float, (unsigned)r[0]), __builtin_bit_cast(float, (unsigned)r[1]));
  auto s = __builtin_amdgcn_permlane32_swap(__builtin_bit_cast(unsigned, a), __builtin_bit_cast(unsigned, a), false, false);
  return fmaxf(__builtin_bit_cast(float, (unsigned)s[0]), __builtin_bit_cast(float, (unsigned)s[1]));
}
__device__ __forceinline__ float xor4(float x) { return dpp<XOR3>(dpp<XOR7>(x)); }
__device__ __forceinline__ float sum8(float x) { x += dpp<XOR7>(x); x += dpp<XOR2>(x); return x + dpp<XOR1>(x); }
__device__ __forceinline__ float max8(float x) {
  float y;
  asm("s_nop 1\n\tv_max_f32_dpp %0, %1, %1 row_half_mirror row_mask:0xf bank_mask:0xf\n\ts_nop 1\n\t"
      "v_max_f32_dpp %0, %0, %0 quad_perm:[2,3,0,1] row_mask:0xf bank_mask:0xf\n\ts_nop 1\n\t"
      "v_max_f32_dpp %0, %0, %0 quad_perm:[1,0,3,2] row_mask:0xf bank_mask:0xf" : "=&v"(y) : "v"(x));
  return y;
}
#define WG_BARRIER() asm volatile("s_waitcnt lgkmcnt(0)\n\ts_barrier" ::: "memory")
#define INFLIGHT(n) asm volatile("s_waitcnt vmcnt(%0)" :: "n"((n) - 1))
#define FENCE() __builtin_amdgcn_sched_barrier(0)
#define RSRC(ptr, bytes) __builtin_amdgcn_make_buffer_rsrc((void*)(ptr), 0, (bytes), 0x00020000)

struct Ptrs {
  const unsigned short* x;
  const unsigned short* k[2];
  const unsigned short* v[2];
  const unsigned short* wq[2];
  const unsigned short* wo[2];
  const unsigned short* gw;
  unsigned short* out;
  unsigned char* ws;
};
static_assert(sizeof(Ptrs) == 96, "");
constexpr int WS_EPOCH = 0;
constexpr int WS_CNT = 4096;
constexpr int WS_QG = WS_CNT + 16 * NP * 256;
constexpr int WS_OG = WS_QG + 16 * 512 * 8;
constexpr int WS_YG = WS_OG + 256 * 512 * 8;
constexpr int WS_END = WS_YG + 256 * DM * 8;
constexpr int SC1 = 16, NT = 2, OOB = 0x7ffffff0;

__global__ void __launch_bounds__(512, 2) gated_mix_decode_kernel(Ptrs) {
  __shared__ __attribute__((aligned(16))) unsigned q_l[512];
  __shared__ __attribute__((aligned(16))) float ol[2][NWAVE][512];
  __shared__ __attribute__((aligned(16))) float ml[2][4][NWAVE];
  __shared__ __attribute__((aligned(16))) unsigned short cvb[17][32];
  __shared__ __attribute__((aligned(16))) float ml2[2][2][NWAVE];
  __shared__ __attribute__((aligned(16))) unsigned misc[8];

  u64 t_entry = __builtin_amdgcn_s_memrealtime();
  Ptrs p;
  {
    typedef unsigned u32x16 __attribute__((ext_vector_type(16)));
    typedef unsigned u32x8 __attribute__((ext_vector_type(8)));
    typedef const __attribute__((address_space(4))) unsigned char kbyte;
    kbyte* ka = (kbyte*)__builtin_amdgcn_kernarg_segment_ptr();
    u32x16 a0 = *(const __attribute__((address_space(4))) u32x16*)ka;
    u32x8 a1 = *(const __attribute__((address_space(4))) u32x8*)(ka + 64);
    asm volatile("" : "+s"(a0), "+s"(a1), "+s"(t_entry));
#define KP(v, i) (((u64)v[2 * (i) + 1] << 32) | v[2 * (i)])
    p.x = (const unsigned short*)KP(a0, 0);
    p.k[0] = (const unsigned short*)KP(a0, 1); p.k[1] = (const unsigned short*)KP(a0, 2);
    p.v[0] = (const unsigned short*)KP(a0, 3); p.v[1] = (const unsigned short*)KP(a0, 4);
    p.wq[0] = (const unsigned short*)KP(a0, 5); p.wq[1] = (const unsigned short*)KP(a0, 6);
    p.wo[0] = (const unsigned short*)KP(a0, 7); p.wo[1] = (const unsigned short*)KP(a1, 0);
    p.gw = (const unsigned short*)KP(a1, 1); p.out = (unsigned short*)KP(a1, 2); p.ws = (unsigned char*)KP(a1, 3);
  }
  const int bid = blockIdx.x;
  const int s = bid & 15, b = (bid >> 4) & 1, kvh = bid >> 5, grp = bid >> 4;
  const int tid = threadIdx.x, lane = tid & 63;
  const int wave = __builtin_amdgcn_readfirstlane(tid >> 6);
  const unsigned tstart = (unsigned)t_entry + (unsigned)(wave * PACE / 128);

  const size_t base = (size_t)(b * HKV + kvh) * SK * DH;
  const __amdgpu_buffer_rsrc_t rka = RSRC(p.k[0] + base, SK * DH * 2), rva = RSRC(p.v[0] + base, SK * DH * 2);
  const __amdgpu_buffer_rsrc_t rkb = RSRC(p.k[1] + base, SK * DH * 2), rvb = RSRC(p.v[1] + base, SK * DH * 2);
  const __amdgpu_buffer_rsrc_t rw = RSRC(p.ws, WS_END);
  const int voff = (lane >> 4) * 512 + (lane & 15) * 16;
  const int voa = voff + (s * 64 + wave) * 4096, vob = voff + (s * 8 * SB + wave) * 4096;
  int vor = OOB;
  u32x4 buf[NS], ring[8], wo[8];
#define SUBOFF(i) ((((i) & 3) >> 1) * 2048 + ((i) & 1) * 256)
  auto issue = [&](auto T) {
    constexpr int t = decltype(T)::value;
    if constexpr (t >= 8 && t < NS) {
      while ((int)((unsigned)__builtin_amdgcn_s_memrealtime() - tstart) < POFF + t * PACE / 16) __builtin_amdgcn_s_sleep(1);
      FENCE();
    }
    if constexpr (t < 32) buf[t] = __builtin_amdgcn_raw_buffer_load_b128(rka, voa, (t >> 2) * 32768 + SUBOFF(t), NT);
    else if constexpr (t < 64) buf[t] = __builtin_amdgcn_raw_buffer_load_b128(rva, voa, ((t - 32) >> 2) * 32768 + SUBOFF(t), NT);
    else if constexpr (t < 64 + 4 * SB) buf[t] = __builtin_amdgcn_raw_buffer_load_b128(rkb, vob, ((t - 64) >> 2) * 32768 + SUBOFF(t), NT);
    else if constexpr (t < NS) buf[t] = __builtin_amdgcn_raw_buffer_load_b128(rvb, vob, ((t - 64 - 4 * SB) >> 2) * 32768 + SUBOFF(t), NT);
    else ring[t - NS] = __builtin_amdgcn_raw_buffer_load_b128(t - NS < 4 ? rkb : rvb, vor, SUBOFF(t - NS), NT);
    FENCE();
  };

  if (wave == 0) {
    const int u = lane >> 2, c4 = lane & 3, jq = ((s & 7) << 1) | (s >> 3);
    const __amdgpu_buffer_rsrc_t rq = RSRC((b ? p.wq[1] : p.wq[0]) + kvh * 512 + jq * 32, 447 * 8192 + 64);
    const __amdgpu_buffer_rsrc_t rx = RSRC(p.x, 2 * DM * 2), rg = RSRC(p.gw, DM * 2);
    const unsigned epoch = __builtin_amdgcn_raw_buffer_load_b32(rw, WS_EPOCH, 0, SC1);
    __builtin_amdgcn_raw_buffer_store_b32(0u, rw, (s == 0 && lane < NP) ? WS_CNT + (grp * NP + lane) * 256 : OOB, 0, SC1);
    u32x4 xv[3][4], w[28];
#pragma unroll
    for (int i = 0; i < 4; ++i) {
      xv[0][i] = __builtin_amdgcn_raw_buffer_load_b128(rx, 56 * u, 16 * i, 0);
      xv[1][i] = __builtin_amdgcn_raw_buffer_load_b128(rx, 56 * u, DM * 2 + 16 * i, 0);
    }
#pragma unroll
    for (int i = 0; i < 28; ++i) w[i] = __builtin_amdgcn_raw_buffer_load_b128(rq, u * (28 * 8192) + c4 * 16, i * 8192, NT);
#pragma unroll
    for (int i = 0; i < 4; ++i) xv[2][i] = __builtin_amdgcn_raw_buffer_load_b128(rg, 56 * u, 16 * i, 0);
    FENCE();
    asm volatile("s_barrier" ::: "memory");
    FENCE();
    const unsigned tag = __builtin_amdgcn_readfirstlane(epoch) + 1u;
    float a[16];
#pragma unroll
    for (int e = 0; e < 16; ++e) a[e] = 0.f;
#pragma unroll
    for (int i = 0; i < 14; ++i)
#pragma unroll
      for (int e = 0; e < 4; ++e) {
        const unsigned lo = __builtin_amdgcn_perm(w[2 * i + 1][e], w[2 * i][e], 0x05040100u);
        const unsigned hi = __builtin_amdgcn_perm(w[2 * i + 1][e], w[2 * i][e], 0x07060302u);
        const unsigned x0 = xv[0][i >> 2][i & 3], x1 = xv[1][i >> 2][i & 3];
        a[2 * e] = dot2(lo, x0, a[2 * e]); a[2 * e + 1] = dot2(hi, x0, a[2 * e + 1]);
        a[8 + 2 * e] = dot2(lo, x1, a[8 + 2 * e]); a[8 + 2 * e + 1] = dot2(hi, x1, a[8 + 2 * e + 1]);
      }
#pragma unroll
    for (int e = 0; e < 8; ++e) a[e] = swap32_add(a[e], a[e + 8]);
#pragma unroll
    for (int e = 0; e < 4; ++e) a[e] = swap16_add(a[e], a[e + 4]);
#pragma unroll
    for (int e = 0; e < 2; ++e) {
      const float t1 = a[e] + dpp<XOR8>(a[e]), t2 = a[e + 2] + dpp<XOR8>(a[e + 2]);
      a[e] = (lane & 8) ? t2 : t1;
      a[e] += xor4(a[e]);
    }
    asm volatile("s_waitcnt vmcnt(0)" ::: "memory");
    __builtin_amdgcn_raw_buffer_store_b64(u32x2{pk_bf16(a[0], a[1]), tag}, rw,
        (lane & 4) ? OOB : WS_QG + (((kvh * 2 + (lane >> 5)) * 2 + b) * 256 + jq * 16 + c4 * 4 + ((lane >> 4) & 1) * 2 + ((lane >> 3) & 1)) * 8, 0, SC1);
    float g = 0.f;
#pragma unroll
    for (int i = 0; i < 14; ++i) g = dot2(b ? xv[1][i >> 2][i & 3] : xv[0][i >> 2][i & 3], xv[2][i >> 2][i & 3], g);
    g = swap32_add(g, g); g = swap16_add(g, g); g += dpp<XOR8>(g); g += xor4(g);
    g = rbf(1.f / (1.f + __expf(-rbf(g))));
    if (lane == 0) { misc[0] = tag; misc[1] = __builtin_bit_cast(unsigned, g); misc[2] = __builtin_bit_cast(unsigned, rbf(1.f - g)); }
    static_for<8>([&](auto T) { if (decltype(T)::value >= 4) INFLIGHT(PCAP); issue(T); });
    for (unsigned spin = 0;; ++spin) {
      bool ok = true;
      u32x2 v[8];
#pragma unroll
      for (int k = 0; k < 8; ++k) v[k] = __builtin_amdgcn_raw_buffer_load_b64(rw, WS_QG + (grp * 512 + lane) * 8, 512 * k, SC1);
      asm volatile("" ::: "memory");
#pragma unroll
      for (int k = 0; k < 8; ++k) { q_l[lane + 64 * k] = v[k][0]; ok &= v[k][1] == tag; }
      if (__all(ok) || spin > SPIN_CAP) break;
      __builtin_amdgcn_s_sleep(1);
    }
    asm volatile("s_waitcnt lgkmcnt(0)" ::: "memory");
    if (lane == 0) __hip_atomic_store(&misc[3], 1u, RLX_WG);
    static_for<PRE - 8>([&](auto T) { if (decltype(T)::value >= 6) INFLIGHT(PCAP); issue(IC<decltype(T)::value + 8>{}); });
#pragma unroll
    for (int i = 0; i < 8; ++i) wo[i] = u32x4{0u, 0u, 0u, 0u};
  } else {
    gcu32x4* wop = (gcu32x4*)(((lane & 8) ? p.wo[1] : p.wo[0]) + (size_t)(kvh * 512 + s * 32 + (lane >> 4) * 8) * DM + (wave - 1) * 64 + (lane & 7) * 8);
    if (wave == 2 && lane < 16) ((unsigned*)cvb[16])[lane] = 0u;
    if (tid < 64 + 2) misc[3 + lane] = 0u;
    static_for<2>([&](auto T) { issue(T); });
    WG_BARRIER();
    FENCE();
    u32x4 w[8];
#pragma unroll
    for (int i = 0; i < 8; ++i) { if (i >= 6) INFLIGHT(PCAP); w[i] = __builtin_nontemporal_load(wop + i * (DM / 8)); FENCE(); }
    static_for<PRE - 2>([&](auto T) { INFLIGHT(PCAP); issue(IC<decltype(T)::value + 2>{}); });
    const bool up = lane & 8;
    unsigned h[2][8][2];
#pragma unroll
    for (int i = 0; i < 8; ++i)
#pragma unroll
      for (int e = 0; e < 2; ++e) {
        const unsigned recv = __builtin_amdgcn_mov_dpp(up ? w[i][e] : w[i][2 + e], XOR8, 0xf, 0xf, true);
        h[0][i][e] = up ? recv : w[i][e];
        h[1][i][e] = up ? w[i][2 + e] : recv;
      }
#pragma unroll
    for (int br = 0; br < 2; ++br)
#pragma unroll
      for (int i = 0; i < 4; ++i)
#pragma unroll
        for (int e = 0; e < 2; ++e) {
          wo[4 * br + 2 * e][i] = __builtin_amdgcn_perm(h[br][2 * i + 1][e], h[br][2 * i][e], 0x05040100u);
          wo[4 * br + 2 * e + 1][i] = __builtin_amdgcn_perm(h[br][2 * i + 1][e], h[br][2 * i][e], 0x07060302u);
        }
#pragma unroll
    for (int e = 0; e < 8; ++e) asm volatile("" : "+v"(wo[e]));
    while (__hip_atomic_load(&misc[3], RLX_WG) == 0u) __builtin_amdgcn_s_sleep(1);
    asm volatile("" ::: "memory");
  }
  FENCE();
  const unsigned tag = misc[0];
  u32x4 qa[2], qb[2];
  auto load_q = [&](int br) {
    const int h = (lane >> 3) & 1, c = lane & 15;
#pragma unroll
    for (int hf = 0; hf < 2; ++hf) {
      qa[hf] = *(const u32x4*)&q_l[br * 256 + h * 128 + hf * 64 + 4 * c];
      qb[hf] = *(const u32x4*)&q_l[br * 256 + (h ^ 1) * 128 + hf * 64 + 4 * c];
    }
  };
  load_q(0);
  unsigned msk[4];
#pragma unroll
  for (int r = 0; r < 4; ++r) msk[r] = (((lane & 7) >> 1) == r) ? ((lane & 1) ? 0xffff0000u : 0x0000ffffu) : 0u;
  constexpr float C2 = 0.0625f * 1.4426950408889634f;
  float m = -INFINITY, l = 0.f;
  f32x4 acc0 = {0.f, 0.f, 0.f, 0.f}, acc1 = {0.f, 0.f, 0.f, 0.f};

  typedef __attribute__((address_space(3))) const u32x4 lu32x4;
  typedef __attribute__((address_space(3))) const float lf32;
  typedef __attribute__((address_space(3))) unsigned short lu16;
  const bool self_v = (tid >> 5) == s;
  int src1 = self_v ? OOB : WS_OG + (((grp * 16 + (tid >> 5)) * 16 + s) * 32 + (lane & 31)) * 8;
  int dst1 = self_v ? OOB : WS_OG + (bid * 512 + tid) * 8;
  const int yl = (bid >> 5) * 8 + (bid & 7);
  const int fb = yl >= 28 ? 1 : 0, cb = (yl - fb * 28) * 4 + ((bid >> 3) & 3);
  int yoff = wave > 0 ? WS_YG + (bid * DM + (wave - 1) * 64 + (lane & 7) * 8 + ((lane >> 3) & 1) * 4 + (lane >> 4)) * 8
                      : WS_YG + ((fb * 16 + (lane >> 2)) * DM + cb * 4 + (lane & 3)) * 8;
  gu32* outp = (gu32*)(p.out + fb * DM + cb * 4 + (lane & 2));
  lu32x4* paa = (lu32x4*)&cvb[(lane & 15) < 8 ? (lane & 15) : 16][(lane >> 4) * 8];
  lu32x4* pab = (lu32x4*)&cvb[(lane & 15) < 8 ? 16 : (lane & 15)][(lane >> 4) * 8];
  lf32* pml = (lf32*)&ml2[lane >> 5][0][(lane & 7) ^ ((lane >> 2) & 4)];
  lu16* pcv = (lu16*)&cvb[(lane >> 5) * 8 + wave][lane & 31];
  float gs = __builtin_bit_cast(float, misc[1 + (lane >> 5)]);
  asm volatile("" : "+v"(src1), "+v"(dst1), "+v"(yoff), "+v"(outp));
  asm volatile("" : "+v"(paa), "+v"(pab), "+v"(pml), "+v"(pcv), "+v"(gs));

  auto park = [&](int br) {
    float ls = swap16_add(l, l); ls = swap32_add(ls, ls);
    float* olw = &ol[br][wave][(lane >> 5) * 256 + 8 * (lane & 15) + ((lane >> 4) & 1) * 4];
    *(f32x4*)olw = acc0;
    *(f32x4*)(olw + 128) = acc1;
    if ((lane & 55) == 0) { ml[br][lane >> 3][wave] = m * C2; ml[br][2 + (lane >> 3)][wave] = ls; }
  };
  auto merge = [&](int br, float& o, unsigned& bits) {
    const float mw = ml[br][wave >> 2][lane & 7], lw = ml[br][2 + (wave >> 2)][lane & 7];
    float ov[NWAVE];
#pragma unroll
    for (int w = 0; w < NWAVE; ++w) ov[w] = ol[br][w][tid];
    const float M = max8(mw);
    const float sw = __builtin_amdgcn_exp2f(mw - M);
    o = dpp<BCAST>(sw) * ov[0];
    static_for<7>([&](auto W) { constexpr int w = decltype(W)::value + 1; o = fmaf(dpp<BCAST + w>(sw), ov[w], o); });
    const unsigned L = __builtin_amdgcn_readfirstlane(__builtin_bit_cast(unsigned, sum8(sw * lw)));
    bits = ((__builtin_bit_cast(unsigned, M) >> (lane & 31)) & 1u) | (((L >> (lane & 31)) & 1u) << 1);
  };
  auto p_operand = [&](float sc, float mc) {
    const float pv = __builtin_amdgcn_exp2f(fmaf(sc, C2, -mc));
    l += pv;
    const unsigned pd = pk_bf16(pv, pv);
    return u32x4{pd & msk[0], pd & msk[1], pd & msk[2], pd & msk[3]};
  };

  float oa = 0.f, xa = 0.f, xb = 0.f, s0 = 0.f, mc = 0.f;
  unsigned abits = 0u, sp[8];
  u32x4 pa;
  auto consume = [&](auto I) {
    constexpr int i = decltype(I)::value;
    constexpr bool isK = i < 32 || (i >= 64 && i < 64 + 4 * SB);
    constexpr int r = isK ? (i < 32 ? i : i - 64) : (i < 64 ? i - 32 : i - 64 - 4 * SB);
    if constexpr (i == 64) {
      park(0);
      asm volatile("s_waitcnt lgkmcnt(0)" ::: "memory");
      if (lane == 0) __hip_atomic_fetch_add(&misc[4], 1u, RLX_WG);
      load_q(1);
      m = -INFINITY; l = 0.f; acc0 = f32x4{0.f, 0.f, 0.f, 0.f}; acc1 = acc0;
    }
    if constexpr (i == 64 + 4 * SB) {
      while (__hip_atomic_load(&misc[4], RLX_WG) < 8u) __builtin_amdgcn_s_sleep(1);
      asm volatile("" ::: "memory");
      merge(0, oa, abits);
      asm volatile("" : "+v"(oa), "+v"(abits));
    }
    if constexpr (isK) {
      constexpr int hf = r & 1;
      if constexpr (hf == 0) { xa = 0.f; xb = 0.f; }
#pragma unroll
      for (int e = 0; e < 4; ++e) { xa = dot2(buf[i][e], qa[hf][e], xa); xb = dot2(buf[i][e], qb[hf][e], xb); }
      if constexpr (hf == 1) {
        const float y = sum8(xa + dpp<XOR8>(xb));
        if constexpr ((r & 2) == 0) { s0 = y; asm volatile("" : "+v"(s0)); }
        else {
          sp[r >> 2] = pk_bf16(s0, y);
          m = fmaxf(m, fmaxf(bf_lo(sp[r >> 2]), bf_hi(sp[r >> 2])));
          asm volatile("" : "+v"(m));
        }
      } else asm volatile("" : "+v"(xa), "+v"(xb));
    } else {
      if constexpr (r == 0) { m = xrow_max(m); mc = m * C2; }
      if constexpr ((r & 1) == 0) {
        pa = p_operand((r & 2) ? bf_hi(sp[r >> 2]) : bf_lo(sp[r >> 2]), mc);
        acc0 = mfma(pa, buf[i], acc0);
        asm volatile("" : "+v"(acc0));
      } else {
        acc1 = mfma(pa, buf[i], acc1);
        asm volatile("" : "+v"(acc1));
      }
    }
  };
  const int pool = (wave + s) & (NP - 1);
  const __amdgpu_buffer_rsrc_t rc = RSRC(p.ws + WS_CNT + (grp * NP + pool) * 256, 4);
  const bool dynw = ((wave + 7) & 7) < DYNW;
  int cl = 0, first = NCH;
  auto claim = [&](bool on) { cl = __builtin_amdgcn_raw_ptr_buffer_atomic_add_i32(1, rc, lane * 4, on ? 0 : 1024, 0); FENCE(); };
  auto claimed = [&]() { asm volatile("" : "+v"(cl)); return DBASE + __builtin_amdgcn_readfirstlane(cl) * NP + pool; };
  static_for<NS + 8 - PRE>([&](auto U) {
    constexpr int t = decltype(U)::value + PRE;
    constexpr int c0 = t == PRE ? 0 : (2 * (t - PRE) < t - 8 ? 2 * (t - PRE) : t - 8);
    constexpr int c1 = 2 * (t - PRE + 1) < t - 7 ? 2 * (t - PRE + 1) : t - 7;
    static_for<c1 - c0>([&](auto K) { consume(IC<c0 + decltype(K)::value>{}); });
    if constexpr (t == NS - 4) claim(dynw);
    if constexpr (t == NS) {
      const int got = claimed();
      first = dynw ? got : NCH;
      vor = voff + ((first < NCH ? first : NCH) << 12);
    }
    if constexpr (t == NS + 4) claim(first < NCH);
    if constexpr (t < PCUNTIL) INFLIGHT(PCAP); else if constexpr (t < NS) INFLIGHT(CAP);
    issue(IC<t>{});
  });

  int cur = claimed();
  auto process = [&](auto ISS) {
    constexpr bool ISSUE = decltype(ISS)::value;
    const int vo = voff + (cur << 12);
    float sc[2];
#pragma unroll
    for (int j = 0; j < 2; ++j) {
      float ya = 0.f, yb = 0.f;
#pragma unroll
      for (int hf = 0; hf < 2; ++hf) {
#pragma unroll
        for (int e = 0; e < 4; ++e) { ya = dot2(ring[2 * j + hf][e], qa[hf][e], ya); yb = dot2(ring[2 * j + hf][e], qb[hf][e], yb); }
        if constexpr (ISSUE) {
          asm volatile("" : "+v"(ya), "+v"(yb));
          ring[2 * j + hf] = __builtin_amdgcn_raw_buffer_load_b128(rkb, vo, SUBOFF(2 * j + hf), NT);
          FENCE();
        }
      }
      sc[j] = sum8(ya + dpp<XOR8>(yb));
    }
    if constexpr (ISSUE) claim(true);
    const unsigned pk = pk_bf16(sc[0], sc[1]);
    sc[0] = bf_lo(pk); sc[1] = bf_hi(pk);
    const float cm = fmaxf(sc[0], sc[1]);
    if (__any(cm > m)) {
      const float mn = fmaxf(m, xrow_max(cm));
      const float al = __builtin_amdgcn_exp2f((m - mn) * C2);
      const float alo = dpp<XOR8>(al);
      const float ad = (((lane >> 3) & 1) == (lane >> 5)) ? al : alo;
      l *= al; m = mn;
      acc0 *= ad; acc1 *= ad;
    }
    const float mcc = m * C2;
#pragma unroll
    for (int j = 0; j < 2; ++j) {
      const u32x4 a = p_operand(sc[j], mcc);
      acc0 = mfma(a, ring[4 + 2 * j], acc0);
      if constexpr (ISSUE) {
        asm volatile("" : "+v"(acc0));
        ring[4 + 2 * j] = __builtin_amdgcn_raw_buffer_load_b128(rvb, vo, SUBOFF(2 * j), NT);
        FENCE();
      }
      acc1 = mfma(a, ring[5 + 2 * j], acc1);
      if constexpr (ISSUE) {
        asm volatile("" : "+v"(acc1));
        ring[5 + 2 * j] = __builtin_amdgcn_raw_buffer_load_b128(rvb, vo, SUBOFF(2 * j + 1), NT);
        FENCE();
      }
    }
  };
  if (cur < NCH && first < NCH) {
#pragma unroll 1
    do {
      process(std::true_type{});
      cur = claimed();
    } while (cur < NCH);
  }
  if (first < NCH) process(std::false_type{});

  park(1);
  const unsigned tag4 = tag << 4;
  WG_BARRIER();
  float ob;
  unsigned bbits;
  merge(1, ob, bbits);
  const u32x2 own = {pk_bf16(oa, ob), tag4 | abits | (bbits << 2)};
  __builtin_amdgcn_raw_buffer_store_b64(own, rw, dst1, 0, SC1);
  FENCE();
  {
    u32x2 v1;
    __builtin_amdgcn_s_sleep(8);
    for (unsigned spin = 0;; ++spin) {
      v1 = __builtin_amdgcn_raw_buffer_load_b64(rw, src1, 0, SC1);
      asm volatile("" ::: "memory");
      v1 = self_v ? own : v1;
      if (__all((v1[1] ^ tag4) < 16u) || spin > SPIN_CAP) break;
    }
    const u64 bma = __ballot(v1[1] & 1u), bla = __ballot(v1[1] & 2u), bmb = __ballot(v1[1] & 4u), blb = __ballot(v1[1] & 8u);
#define LO(b) __builtin_bit_cast(float, (unsigned)(b))
#define HI(b) __builtin_bit_cast(float, (unsigned)((b) >> 32))
    const bool lo = lane < 32;
    const float mxa = fmaxf(LO(bma), HI(bma)), mxb = fmaxf(LO(bmb), HI(bmb));
    const float ea = __builtin_amdgcn_exp2f((lo ? LO(bma) : HI(bma)) - mxa), eb = __builtin_amdgcn_exp2f((lo ? LO(bmb) : HI(bmb)) - mxb);
    const float cx = swap32_add(bf_lo(v1[0]) * ea, bf_hi(v1[0]) * eb);
    *pcv = (unsigned short)pk_bf16(cx, cx);
    const float l2 = swap32_add((lo ? LO(bla) : HI(bla)) * ea, (lo ? LO(blb) : HI(blb)) * eb);
    if ((lane & 31) == 0) { ml2[lane >> 5][0][wave] = lo ? mxa : mxb; ml2[lane >> 5][1][wave] = l2; }
  }
  WG_BARRIER();
  if (wave > 0) {
    const float mw = pml[0], lw = pml[NWAVE];
    const u32x4 aa = *paa, ab = *pab;
    FENCE();
    f32x4 d[4];
#pragma unroll
    for (int t = 0; t < 4; ++t) d[t] = mfma(aa, wo[t], f32x4{0.f, 0.f, 0.f, 0.f});
#pragma unroll
    for (int t = 0; t < 4; ++t) d[t] = mfma(ab, wo[4 + t], d[t]);
    const float sw = __builtin_amdgcn_exp2f(mw - max8(mw));
    const float wt = sw * gs * __builtin_amdgcn_rcpf(sum8(sw * lw));
    const float w0 = dpp<BCAST>(wt), w1 = dpp<BCAST + 1>(wt), w2 = dpp<BCAST + 2>(wt), w3 = dpp<BCAST + 3>(wt);
    float a[4];
#pragma unroll
    for (int t = 0; t < 4; ++t) a[t] = fmaf(d[t][3], w3, fmaf(d[t][2], w2, fmaf(d[t][1], w1, d[t][0] * w0)));
    const float y = swap16_add(swap32_add(a[0], a[2]), swap32_add(a[1], a[3]));
    __builtin_amdgcn_raw_buffer_store_b64(u32x2{__builtin_bit_cast(unsigned, y), tag}, rw, yoff, 0, SC1);
  } else if (bid < 224) {
    u32x2 v[8];
#pragma unroll
    for (int k = 0; k < 8; ++k) v[k] = u32x2{0u, ~tag};
    { unsigned unused = *(volatile gu32*)outp; asm volatile("" :: "v"(unused)); }
    __builtin_amdgcn_s_sleep(24);
    unsigned pend = 0xffu;
    for (unsigned spin = 0; pend != 0u && spin < SPIN_CAP; ++spin) {
#pragma unroll
      for (int k = 0; k < 8; ++k)
        if (pend >> k & 1u) v[k] = __builtin_amdgcn_raw_buffer_load_b64(rw, yoff, k * 32 * DM * 8, SC1);
      asm volatile("" ::: "memory");
      pend = 0u;
#pragma unroll
      for (int k = 0; k < 8; ++k) pend |= __all(v[k][1] == tag) ? 0u : 1u << k;
    }
#define GV(k) __builtin_bit_cast(float, v[k][0])
    float y = ((GV(0) + GV(1)) + (GV(2) + GV(3))) + ((GV(4) + GV(5)) + (GV(6) + GV(7)));
    y = swap32_add(y, y); y = swap16_add(y, y); y += dpp<XOR8>(y); y += xor4(y);
    const unsigned pr = pk_bf16(y, dpp<XOR1>(y));
    if ((lane & 61) == 0) __hip_atomic_store(outp, pr, RLX_AGENT);
    if (bid == 0 && lane == 0) __hip_atomic_store((gu32*)(p.ws + WS_EPOCH), tag, RLX_AGENT);
  }
}

extern "C" void kernel_launch(void* const* d_in, const int* in_sizes, int n_in, void* d_out, int out_size, void* d_ws, size_t ws_size,
                              hipStream_t stream) {
  Ptrs p;
  p.x = (const unsigned short*)d_in[0];
  p.k[0] = (const unsigned short*)d_in[1]; p.v[0] = (const unsigned short*)d_in[2];
  p.wq[0] = (const unsigned short*)d_in[3]; p.wo[0] = (const unsigned short*)d_in[4];
  p.k[1] = (const unsigned short*)d_in[6]; p.v[1] = (const unsigned short*)d_in[7];
  p.wq[1] = (const unsigned short*)d_in[8]; p.wo[1] = (const unsigned short*)d_in[9];
  p.gw = (const unsigned short*)d_in[10];
  p.out = (unsigned short*)d_out; p.ws = (unsigned char*)d_ws;
  gated_mix_decode_kernel<<<256, 512, 0, stream>>>(p);
}
```

```cpp
#include <hip/hip_runtime.h>
#include <hip/hip_bf16.h>
#include <stdint.h>
#include <utility>

typedef unsigned u32x4 __attribute__((ext_vector_type(4)));
typedef unsigned u32x2 __attribute__((ext_vector_type(2)));
typedef float f32x4 __attribute__((ext_vector_type(4)));
typedef float f32x2 __attribute__((ext_vector_type(2)));
typedef short bf16x8 __attribute__((ext_vector_type(8)));
typedef __bf16 bf16x2 __attribute__((ext_vector_type(2)));
typedef unsigned long long u64;
typedef __attribute__((address_space(1))) unsigned gu32;
typedef __attribute__((address_space(1))) const u32x4 gcu32x4;
#define RLX_AGENT __ATOMIC_RELAXED, __HIP_MEMORY_SCOPE_AGENT
#define RLX_WG __ATOMIC_RELAXED, __HIP_MEMORY_SCOPE_WORKGROUP

constexpr int DM = 448, HKV = 8, SK = 8192, DH = 256, NWAVE = 8;
constexpr int NCH = 1024;
constexpr int SB = 6;
constexpr int NS = 64 + 8 * SB;
constexpr int DBASE = 128 * SB;
constexpr int NP = 2;
constexpr int DYNW = 4;
constexpr int PRE = 20;
constexpr int CAP = 3, PCAP = 4, PCUNTIL = 32;
constexpr int PACE = 444, POFF = 150;
constexpr unsigned SPIN_CAP = 1u << 18;

__device__ __forceinline__ int chunk_pos(int n, int s, int slot) { return ((s >> 1) << 7) | (n << 4) | ((s & 1) << 3) | slot; }
__device__ __forceinline__ int claimed_off(int c) { return (c < NCH ? chunk_pos(c >> 7, (c >> 3) & 15, c & 7) : NCH) << 12; }

template <int... I, class F> __device__ __forceinline__ void static_for_impl(std::integer_sequence<int, I...>, F&& f) { (f(std::integral_constant<int, I>{}), ...); }
template <int N, class F> __device__ __forceinline__ void static_for(F&& f) { static_for_impl(std::make_integer_sequence<int, N>{}, f); }
template <int N> using IC = std::integral_constant<int, N>;

__device__ __forceinline__ float dot2(unsigned a, unsigned b, float acc) {
  return __builtin_amdgcn_fdot2_f32_bf16(__builtin_bit_cast(bf16x2, a), __builtin_bit_cast(bf16x2, b), acc, false);
}
__device__ __forceinline__ f32x4 mfma(u32x4 a, u32x4 b, f32x4 c) {
  return __builtin_amdgcn_mfma_f32_16x16x32_bf16(__builtin_bit_cast(bf16x8, a), __builtin_bit_cast(bf16x8, b), c, 0, 0, 0);
}
template <int CTRL> __device__ __forceinline__ float dpp(float x) {
  return __builtin_bit_cast(float, __builtin_amdgcn_mov_dpp(__builtin_bit_cast(int, x), CTRL, 0xf, 0xf, true));
}
constexpr int XOR1 = 0xB1, XOR2 = 0x4E, XOR3 = 0x1B, XOR7 = 0x141, XOR8 = 0x128, BCAST = 0x150;
__device__ __forceinline__ float bf_lo(unsigned u) { return __builtin_bit_cast(float, u << 16); }
__device__ __forceinline__ float bf_hi(unsigned u) { return __builtin_bit_cast(float, u & 0xffff0000u); }
__device__ __forceinline__ unsigned pk_bf16(float lo, float hi) {
  return __builtin_bit_cast(unsigned, __builtin_convertvector((f32x2){lo, hi}, bf16x2));
}
__device__ __forceinline__ float rbf(float f) { return bf_lo(pk_bf16(f, f)); }
__device__ __forceinline__ float swap16_add(float a, float b) {
  auto r = __builtin_amdgcn_permlane16_swap(__builtin_bit_cast(unsigned, a), __builtin_bit_cast(unsigned, b), false, false);
  return __builtin_bit_cast(float, (unsigned)r[0]) + __builtin_bit_cast(float, (unsigned)r[1]);
}
__device__ __forceinline__ float swap32_add(float a, float b) {
  auto r = __builtin_amdgcn_permlane32_swap(__builtin_bit_cast(unsigned, a), __builtin_bit_cast(unsigned, b), false, false);
  return __builtin_bit_cast(float, (unsigned)r[0]) + __builtin_bit_cast(float, (unsigned)r[1]);
}
__device__ __forceinline__ float xrow_max(float a) {
  auto r = __builtin_amdgcn_permlane16_swap(__builtin_bit_cast(unsigned, a), __builtin_bit_cast(unsigned, a), false, false);
  a = fmaxf(__builtin_bit_cast(float, (unsigned)r[0]), __builtin_bit_cast(float, (unsigned)r[1]));
  auto s = __builtin_amdgcn_permlane32_swap(__builtin_bit_cast(unsigned, a), __builtin_bit_cast(unsigned, a), false, false);
  return fmaxf(__builtin_bit_cast(float, (unsigned)s[0]), __builtin_bit_cast(float, (unsigned)s[1]));
}
__device__ __forceinline__ float xor4(float x) { return dpp<XOR3>(dpp<XOR7>(x)); }
__device__ __forceinline__ float sum8(float x) { x += dpp<XOR7>(x); x += dpp<XOR2>(x); return x + dpp<XOR1>(x); }
__device__ __forceinline__ float max8(float x) {
  float y;
  asm("s_nop 1\n\tv_max_f32_dpp %0, %1, %1 row_half_mirror row_mask:0xf bank_mask:0xf\n\ts_nop 1\n\t"
      "v_max_f32_dpp %0, %0, %0 quad_perm:[2,3,0,1] row_mask:0xf bank_mask:0xf\n\ts_nop 1\n\t"
      "v_max_f32_dpp %0, %0, %0 quad_perm:[1,0,3,2] row_mask:0xf bank_mask:0xf" : "=&v"(y) : "v"(x));
  return y;
}
#define WG_BARRIER() asm volatile("s_waitcnt lgkmcnt(0)\n\ts_barrier" ::: "memory")
#define INFLIGHT(n) asm volatile("s_waitcnt vmcnt(%0)" :: "n"((n) - 1))
#define FENCE() __builtin_amdgcn_sched_barrier(0)
#define RSRC(ptr, bytes) __builtin_amdgcn_make_buffer_rsrc((void*)(ptr), 0, (bytes), 0x00020000)

struct Ptrs {
  const unsigned short* x;
  const unsigned short* k[2];
  const unsigned short* v[2];
  const unsigned short* wq[2];
  const unsigned short* wo[2];
  const unsigned short* gw;
  unsigned short* out;
  unsigned char* ws;
};
static_assert(sizeof(Ptrs) == 96, "");
constexpr int WS_EPOCH = 0;
constexpr int WS_CNT = 4096;
constexpr int WS_QG = WS_CNT + 16 * NP * 256;
constexpr int WS_OG = WS_QG + 16 * 512 * 8;
constexpr int WS_YG = WS_OG + 256 * 512 * 8;
constexpr int WS_END = WS_YG + 256 * DM * 8;
constexpr int SC1 = 16, NT = 2, OOB = 0x7ffffff0;

__global__ void __launch_bounds__(512, 2) gated_mix_decode_kernel(Ptrs) {
  __shared__ __attribute__((aligned(16))) unsigned q_l[512];
  __shared__ __attribute__((aligned(16))) float ol[2][NWAVE][512];
  __shared__ __attribute__((aligned(16))) float ml[2][4][NWAVE];
  __shared__ __attribute__((aligned(16))) unsigned short cvb[17][32];
  __shared__ __attribute__((aligned(16))) float ml2[2][2][NWAVE];
  __shared__ __attribute__((aligned(16))) unsigned misc[8];

  u64 t_entry = __builtin_amdgcn_s_memrealtime();
  Ptrs p;
  {
    typedef unsigned u32x16 __attribute__((ext_vector_type(16)));
    typedef unsigned u32x8 __attribute__((ext_vector_type(8)));
    typedef const __attribute__((address_space(4))) unsigned char kbyte;
    kbyte* ka = (kbyte*)__builtin_amdgcn_kernarg_segment_ptr();
    u32x16 a0 = *(const __attribute__((address_space(4))) u32x16*)ka;
    u32x8 a1 = *(const __attribute__((address_space(4))) u32x8*)(ka + 64);
    asm volatile("" : "+s"(a0), "+s"(a1), "+s"(t_entry));
#define KP(v, i) (((u64)v[2 * (i) + 1] << 32) | v[2 * (i)])
    p.x = (const unsigned short*)KP(a0, 0);
    p.k[0] = (const unsigned short*)KP(a0, 1); p.k[1] = (const unsigned short*)KP(a0, 2);
    p.v[0] = (const unsigned short*)KP(a0, 3); p.v[1] = (const unsigned short*)KP(a0, 4);
    p.wq[0] = (const unsigned short*)KP(a0, 5); p.wq[1] = (const unsigned short*)KP(a0, 6);
    p.wo[0] = (const unsigned short*)KP(a0, 7); p.wo[1] = (const unsigned short*)KP(a1, 0);
    p.gw = (const unsigned short*)KP(a1, 1); p.out = (unsigned short*)KP(a1, 2); p.ws = (unsigned char*)KP(a1, 3);
  }
  const int bid = blockIdx.x;
  const int s = bid & 15, b = (bid >> 4) & 1, kvh = bid >> 5, grp = bid >> 4;
  const int tid = threadIdx.x, lane = tid & 63;
  const int wave = __builtin_amdgcn_readfirstlane(tid >> 6);
  const unsigned tstart = (unsigned)t_entry + (unsigned)(wave * PACE / 128);

  const size_t base = (size_t)(b * HKV + kvh) * SK * DH;
  const __amdgpu_buffer_rsrc_t rka = RSRC(p.k[0] + base, SK * DH * 2), rva = RSRC(p.v[0] + base, SK * DH * 2);
  const __amdgpu_buffer_rsrc_t rkb = RSRC(p.k[1] + base, SK * DH * 2), rvb = RSRC(p.v[1] + base, SK * DH * 2);
  const __amdgpu_buffer_rsrc_t rw = RSRC(p.ws, WS_END);
  const int r8 = wave & 1;
  int vq[4];
#pragma unroll
  for (int i = 0; i < 4; ++i) vq[i] = (i >> 1) * 2048 + (lane >> 4) * 512 + ((i & 1) ^ r8) * 256 + (lane & 15) * 16;
  const int sto = chunk_pos(0, s, wave ^ ((s ^ grp) & 7)) << 12;
  int vor = NCH << 12;
  u32x4 buf[NS], ring[8], wo[8];
  auto issue = [&](auto T) {
    constexpr int t = decltype(T)::value;
    if constexpr (t >= 8 && t < NS) {
      while ((int)((unsigned)__builtin_amdgcn_s_memrealtime() - tstart) < POFF + t * PACE / 16) __builtin_amdgcn_s_sleep(1);
      FENCE();
    }
    if constexpr (t < 32) buf[t] = __builtin_amdgcn_raw_buffer_load_b128(rka, vq[t & 3], sto + (t >> 2) * 65536, NT);
    else if constexpr (t < 64) buf[t] = __builtin_amdgcn_raw_buffer_load_b128(rva, vq[t & 3], sto + ((t - 32) >> 2) * 65536, NT);
    else if constexpr (t < 64 + 4 * SB) buf[t] = __builtin_amdgcn_raw_buffer_load_b128(rkb, vq[t & 3], sto + ((t - 64) >> 2) * 65536, NT);
    else if constexpr (t < NS) buf[t] = __builtin_amdgcn_raw_buffer_load_b128(rvb, vq[t & 3], sto + ((t - 64 - 4 * SB) >> 2) * 65536, NT);
    else ring[t - NS] = __builtin_amdgcn_raw_buffer_load_b128(t - NS < 4 ? rkb : rvb, vq[t & 3], vor, NT);
    FENCE();
  };

  if (wave == 0) {
    const int u = lane >> 2, c4 = lane & 3, jq = ((s & 7) << 1) | (s >> 3);
    const __amdgpu_buffer_rsrc_t rq = RSRC((b ? p.wq[1] : p.wq[0]) + kvh * 512 + jq * 32, 447 * 8192 + 64);
    const __amdgpu_buffer_rsrc_t rx = RSRC(p.x, 2 * DM * 2), rg = RSRC(p.gw, DM * 2);
    const unsigned epoch = __builtin_amdgcn_raw_buffer_load_b32(rw, WS_EPOCH, 0, SC1);
    __builtin_amdgcn_raw_buffer_store_b32(0u, rw, (s == 0 && lane < NP) ? WS_CNT + (grp * NP + lane) * 256 : OOB, 0, SC1);
    u32x4 xv[3][4], w[28];
#pragma unroll
    for (int i = 0; i < 4; ++i) {
      xv[0][i] = __builtin_amdgcn_raw_buffer_load_b128(rx, 56 * u, 16 * i, 0);
      xv[1][i] = __builtin_amdgcn_raw_buffer_load_b128(rx, 56 * u, DM * 2 + 16 * i, 0);
    }
#pragma unroll
    for (int i = 0; i < 28; ++i) w[i] = __builtin_amdgcn_raw_buffer_load_b128(rq, u * (28 * 8192) + c4 * 16, i * 8192, NT);
#pragma unroll
    for (int i = 0; i < 4; ++i) xv[2][i] = __builtin_amdgcn_raw_buffer_load_b128(rg, 56 * u, 16 * i, 0);
    FENCE();
    asm volatile("s_barrier" ::: "memory");
    FENCE();
    const unsigned tag = __builtin_amdgcn_readfirstlane(epoch) + 1u;
    float a[16];
#pragma unroll
    for (int e = 0; e < 16; ++e) a[e] = 0.f;
#pragma unroll
    for (int i = 0; i < 14; ++i)
#pragma unroll
      for (int e = 0; e < 4; ++e) {
        const unsigned lo = __builtin_amdgcn_perm(w[2 * i + 1][e], w[2 * i][e], 0x05040100u);
        const unsigned hi = __builtin_amdgcn_perm(w[2 * i + 1][e], w[2 * i][e], 0x07060302u);
        const unsigned x0 = xv[0][i >> 2][i & 3], x1 = xv[1][i >> 2][i & 3];
        a[2 * e] = dot2(lo, x0, a[2 * e]); a[2 * e + 1] = dot2(hi, x0, a[2 * e + 1]);
        a[8 + 2 * e] = dot2(lo, x1, a[8 + 2 * e]); a[8 + 2 * e + 1] = dot2(hi, x1, a[8 + 2 * e + 1]);
      }
#pragma unroll
    for (int e = 0; e < 8; ++e) a[e] = swap32_add(a[e], a[e + 8]);
#pragma unroll
    for (int e = 0; e < 4; ++e) a[e] = swap16_add(a[e], a[e + 4]);
#pragma unroll
    for (int e = 0; e < 2; ++e) {
      const float t1 = a[e] + dpp<XOR8>(a[e]), t2 = a[e + 2] + dpp<XOR8>(a[e + 2]);
      a[e] = (lane & 8) ? t2 : t1;
      a[e] += xor4(a[e]);
    }
    asm volatile("s_waitcnt vmcnt(0)" ::: "memory");
    __builtin_amdgcn_raw_buffer_store_b64(u32x2{pk_bf16(a[0], a[1]), tag}, rw,
        (lane & 4) ? OOB : WS_QG + (((kvh * 2 + (lane >> 5)) * 2 + b) * 256 + jq * 16 + c4 * 4 + ((lane >> 4) & 1) * 2 + ((lane >> 3) & 1)) * 8, 0, SC1);
    float g = 0.f;
#pragma unroll
    for (int i = 0; i < 14; ++i) g = dot2(b ? xv[1][i >> 2][i & 3] : xv[0][i >> 2][i & 3], xv[2][i >> 2][i & 3], g);
    g = swap32_add(g, g); g = swap16_add(g, g); g += dpp<XOR8>(g); g += xor4(g);
    g = rbf(1.f / (1.f + __expf(-rbf(g))));
    if (lane == 0) { misc[0] = tag; misc[1] = __builtin_bit_cast(unsigned, g); misc[2] = __builtin_bit_cast(unsigned, rbf(1.f - g)); }
    static_for<8>([&](auto T) { if (decltype(T)::value >= 4) INFLIGHT(PCAP); issue(T); });
    for (unsigned spin = 0;; ++spin) {
      bool ok = true;
      u32x2 v[8];
#pragma unroll
      for (int k = 0; k < 8; ++k) v[k] = __builtin_amdgcn_raw_buffer_load_b64(rw, WS_QG + (grp * 512 + lane) * 8, 512 * k, SC1);
      asm volatile("" ::: "memory");
#pragma unroll
      for (int k = 0; k < 8; ++k) { q_l[lane + 64 * k] = v[k][0]; ok &= v[k][1] == tag; }
      if (__all(ok) || spin > SPIN_CAP) break;
      __builtin_amdgcn_s_sleep(1);
    }
    asm volatile("s_waitcnt lgkmcnt(0)" ::: "memory");
    if (lane == 0) __hip_atomic_store(&misc[3], 1u, RLX_WG);
    static_for<PRE - 8>([&](auto T) { if (decltype(T)::value >= 6) INFLIGHT(PCAP); issue(IC<decltype(T)::value + 8>{}); });
#pragma unroll
    for (int i = 0; i < 8; ++i) wo[i] = u32x4{0u, 0u, 0u, 0u};
  } else {
    gcu32x4* wop = (gcu32x4*)(((lane & 8) ? p.wo[1] : p.wo[0]) + (size_t)(kvh * 512 + s * 32 + (lane >> 4) * 8) * DM + (wave - 1) * 64 + (lane & 7) * 8);
    if (wave == 2 && lane < 16) ((unsigned*)cvb[16])[lane] = 0u;
    if (tid < 64 + 2) misc[3 + lane] = 0u;
    static_for<2>([&](auto T) { issue(T); });
    WG_BARRIER();
    FENCE();
    u32x4 w[8];
#pragma unroll
    for (int i = 0; i < 8; ++i) { if (i >= 6) INFLIGHT(PCAP); w[i] = __builtin_nontemporal_load(wop + i * (DM / 8)); FENCE(); }
    static_for<PRE - 2>([&](auto T) { INFLIGHT(PCAP); issue(IC<decltype(T)::value + 2>{}); });
    const bool up = lane & 8;
    unsigned h[2][8][2];
#pragma unroll
    for (int i = 0; i < 8; ++i)
#pragma unroll
      for (int e = 0; e < 2; ++e) {
        const unsigned recv = __builtin_amdgcn_mov_dpp(up ? w[i][e] : w[i][2 + e], XOR8, 0xf, 0xf, true);
        h[0][i][e] = up ? recv : w[i][e];
        h[1][i][e] = up ? w[i][2 + e] : recv;
      }
#pragma unroll
    for (int br = 0; br < 2; ++br)
#pragma unroll
      for (int i = 0; i < 4; ++i)
#pragma unroll
        for (int e = 0; e < 2; ++e) {
          wo[4 * br + 2 * e][i] = __builtin_amdgcn_perm(h[br][2 * i + 1][e], h[br][2 * i][e], 0x05040100u);
          wo[4 * br + 2 * e + 1][i] = __builtin_amdgcn_perm(h[br][2 * i + 1][e], h[br][2 * i][e], 0x07060302u);
        }
#pragma unroll
    for (int e = 0; e < 8; ++e) asm volatile("" : "+v"(wo[e]));
    while (__hip_atomic_load(&misc[3], RLX_WG) == 0u) __builtin_amdgcn_s_sleep(1);
    asm volatile("" ::: "memory");
  }
  FENCE();
  const unsigned tag = misc[0];
  u32x4 qa[2], qb[2];
  auto load_q = [&](int br) {
    const int h = (lane >> 3) & 1, c = lane & 15;
#pragma unroll
    for (int hf = 0; hf < 2; ++hf) {
      qa[hf] = *(const u32x4*)&q_l[br * 256 + h * 128 + (hf ^ r8) * 64 + 4 * c];
      qb[hf] = *(const u32x4*)&q_l[br * 256 + (h ^ 1) * 128 + (hf ^ r8) * 64 + 4 * c];
    }
  };
  load_q(0);
  unsigned msk[4];
#pragma unroll
  for (int r = 0; r < 4; ++r) msk[r] = (((lane & 7) >> 1) == r) ? ((lane & 1) ? 0xffff0000u : 0x0000ffffu) : 0u;
  constexpr float C2 = 0.0625f * 1.4426950408889634f;
  float m = -INFINITY, l = 0.f;
  f32x4 acc0 = {0.f, 0.f, 0.f, 0.f}, acc1 = {0.f, 0.f, 0.f, 0.f};

  typedef __attribute__((address_space(3))) const u32x4 lu32x4;
  typedef __attribute__((address_space(3))) const float lf32;
  typedef __attribute__((address_space(3))) unsigned short lu16;
  const bool self_v = (tid >> 5) == s;
  int src1 = self_v ? OOB : WS_OG + (((grp * 16 + (tid >> 5)) * 16 + s) * 32 + (lane & 31)) * 8;
  int dst1 = self_v ? OOB : WS_OG + (bid * 512 + tid) * 8;
  const int yl = (bid >> 5) * 8 + (bid & 7);
  const int fb = yl >= 28 ? 1 : 0, cb = (yl - fb * 28) * 4 + ((bid >> 3) & 3);
  int yoff = wave > 0 ? WS_YG + (bid * DM + (wave - 1) * 64 + (lane & 7) * 8 + ((lane >> 3) & 1) * 4 + (lane >> 4)) * 8
                      : WS_YG + ((fb * 16 + (lane >> 2)) * DM + cb * 4 + (lane & 3)) * 8;
  gu32* outp = (gu32*)(p.out + fb * DM + cb * 4 + (lane & 2));
  lu32x4* paa = (lu32x4*)&cvb[(lane & 15) < 8 ? (lane & 15) : 16][(lane >> 4) * 8];
  lu32x4* pab = (lu32x4*)&cvb[(lane & 15) < 8 ? 16 : (lane & 15)][(lane >> 4) * 8];
  lf32* pml = (lf32*)&ml2[lane >> 5][0][(lane & 7) ^ ((lane >> 2) & 4)];
  lu16* pcv = (lu16*)&cvb[(lane >> 5) * 8 + wave][lane & 31];
  float gs = __builtin_bit_cast(float, misc[1 + (lane >> 5)]);
  asm volatile("" : "+v"(src1), "+v"(dst1), "+v"(yoff), "+v"(outp));
  asm volatile("" : "+v"(paa), "+v"(pab), "+v"(pml), "+v"(pcv), "+v"(gs));

  auto park = [&](int br) {
    float ls = swap16_add(l, l); ls = swap32_add(ls, ls);
    float* olw = &ol[br][wave][(lane >> 5) * 256 + 8 * (lane & 15) + ((lane >> 4) & 1) * 4];
    *(f32x4*)(olw + r8 * 128) = acc0;
    *(f32x4*)(olw + (r8 ^ 1) * 128) = acc1;
    if ((lane & 55) == 0) { ml[br][lane >> 3][wave] = m * C2; ml[br][2 + (lane >> 3)][wave] = ls; }
  };
  auto merge = [&](int br, float& o, unsigned& bits) {
    const float mw = ml[br][wave >> 2][lane & 7], lw = ml[br][2 + (wave >> 2)][lane & 7];
    float ov[NWAVE];
#pragma unroll
    for (int w = 0; w < NWAVE; ++w) ov[w] = ol[br][w][tid];
    const float M = max8(mw);
    const float sw = __builtin_amdgcn_exp2f(mw - M);
    o = dpp<BCAST>(sw) * ov[0];
    static_for<7>([&](auto W) { constexpr int w = decltype(W)::value + 1; o = fmaf(dpp<BCAST + w>(sw), ov[w], o); });
    const unsigned L = __builtin_amdgcn_readfirstlane(__builtin_bit_cast(unsigned, sum8(sw * lw)));
    bits = ((__builtin_bit_cast(unsigned, M) >> (lane & 31)) & 1u) | (((L >> (lane & 31)) & 1u) << 1);
  };
  auto p_operand = [&](float sc, float mc) {
    const float pv = __builtin_amdgcn_exp2f(fmaf(sc, C2, -mc));
    l += pv;
    const unsigned pd = pk_bf16(pv, pv);
    return u32x4{pd & msk[0], pd & msk[1], pd & msk[2], pd & msk[3]};
  };

  float oa = 0.f, xa = 0.f, xb = 0.f, s0 = 0.f, mc = 0.f;
  unsigned abits = 0u, sp[8];
  u32x4 pa;
  auto consume = [&](auto I) {
    constexpr int i = decltype(I)::value;
    constexpr bool isK = i < 32 || (i >= 64 && i < 64 + 4 * SB);
    constexpr int r = isK ? (i < 32 ? i : i - 64) : (i < 64 ? i - 32 : i - 64 - 4 * SB);
    if constexpr (i == 64) {
      park(0);
      asm volatile("s_waitcnt lgkmcnt(0)" ::: "memory");
      if (lane == 0) __hip_atomic_fetch_add(&misc[4], 1u, RLX_WG);
      load_q(1);
      m = -INFINITY; l = 0.f; acc0 = f32x4{0.f, 0.f, 0.f, 0.f}; acc1 = acc0;
    }
    if constexpr (i == 64 + 4 * SB) {
      while (__hip_atomic_load(&misc[4], RLX_WG) < 8u) __builtin_amdgcn_s_sleep(1);
      asm volatile("" ::: "memory");
      merge(0, oa, abits);
      asm volatile("" : "+v"(oa), "+v"(abits));
    }
    if constexpr (isK) {
      constexpr int hf = r & 1;
      if constexpr (hf == 0) { xa = 0.f; xb = 0.f; }
#pragma unroll
      for (int e = 0; e < 4; ++e) { xa = dot2(buf[i][e], qa[hf][e], xa); xb = dot2(buf[i][e], qb[hf][e], xb); }
      if constexpr (hf == 1) {
        const float y = sum8(xa + dpp<XOR8>(xb));
        if constexpr ((r & 2) == 0) { s0 = y; asm volatile("" : "+v"(s0)); }
        else {
          sp[r >> 2] = pk_bf16(s0, y);
          m = fmaxf(m, fmaxf(bf_lo(sp[r >> 2]), bf_hi(sp[r >> 2])));
          asm volatile("" : "+v"(m));
        }
      } else asm volatile("" : "+v"(xa), "+v"(xb));
    } else {
      if constexpr (r == 0) { m = xrow_max(m); mc = m * C2; }
      if constexpr ((r & 1) == 0) {
        pa = p_operand((r & 2) ? bf_hi(sp[r >> 2]) : bf_lo(sp[r >> 2]), mc);
        acc0 = mfma(pa, buf[i], acc0);
        asm volatile("" : "+v"(acc0));
      } else {
        acc1 = mfma(pa, buf[i], acc1);
        asm volatile("" : "+v"(acc1));
      }
    }
  };
  const int pool = (wave + s) & (NP - 1);
  const __amdgpu_buffer_rsrc_t rc = RSRC(p.ws + WS_CNT + (grp * NP + pool) * 256, 4);
  const bool dynw = ((wave + 7) & 7) < DYNW;
  int cl = 0, first = NCH;
  auto claim = [&](bool on) { cl = __builtin_amdgcn_raw_ptr_buffer_atomic_add_i32(1, rc, lane * 4, on ? 0 : 1024, 0); FENCE(); };
  auto claimed = [&]() { asm volatile("" : "+v"(cl)); return DBASE + __builtin_amdgcn_readfirstlane(cl) * NP + pool; };
  static_for<NS + 8 - PRE>([&](auto U) {
    constexpr int t = decltype(U)::value + PRE;
    constexpr int c0 = t == PRE ? 0 : (2 * (t - PRE) < t - 8 ? 2 * (t - PRE) : t - 8);
    constexpr int c1 = 2 * (t - PRE + 1) < t - 7 ? 2 * (t - PRE + 1) : t - 7;
    static_for<c1 - c0>([&](auto K) { consume(IC<c0 + decltype(K)::value>{}); });
    if constexpr (t == NS - 4) claim(dynw);
    if constexpr (t == NS) {
      const int got = claimed();
      first = dynw ? got : NCH;
      vor = claimed_off(first);
    }
    if constexpr (t == NS + 4) claim(first < NCH);
    if constexpr (t < PCUNTIL) INFLIGHT(PCAP); else if constexpr (t < NS) INFLIGHT(CAP);
    issue(IC<t>{});
  });

  int cur = claimed();
  auto process = [&](auto ISS) {
    constexpr bool ISSUE = decltype(ISS)::value;
    const int vo = claimed_off(cur);
    float sc[2];
#pragma unroll
    for (int j = 0; j < 2; ++j) {
      float ya = 0.f, yb = 0.f;
#pragma unroll
      for (int hf = 0; hf < 2; ++hf) {
#pragma unroll
        for (int e = 0; e < 4; ++e) { ya = dot2(ring[2 * j + hf][e], qa[hf][e], ya); yb = dot2(ring[2 * j + hf][e], qb[hf][e], yb); }
        if constexpr (ISSUE) {
          asm volatile("" : "+v"(ya), "+v"(yb));
          ring[2 * j + hf] = __builtin_amdgcn_raw_buffer_load_b128(rkb, vq[2 * j + hf], vo, NT);
          FENCE();
        }
      }
      sc[j] = sum8(ya + dpp<XOR8>(yb));
    }
    if constexpr (ISSUE) claim(true);
    const unsigned pk = pk_bf16(sc[0], sc[1]);
    sc[0] = bf_lo(pk); sc[1] = bf_hi(pk);
    const float cm = fmaxf(sc[0], sc[1]);
    if (__any(cm > m)) {
      const float mn = fmaxf(m, xrow_max(cm));
      const float al = __builtin_amdgcn_exp2f((m - mn) * C2);
      const float alo = dpp<XOR8>(al);
      const float ad = (((lane >> 3) & 1) == (lane >> 5)) ? al : alo;
      l *= al; m = mn;
      acc0 *= ad; acc1 *= ad;
    }
    const float mcc = m * C2;
#pragma unroll
    for (int j = 0; j < 2; ++j) {
      const u32x4 a = p_operand(sc[j], mcc);
      acc0 = mfma(a, ring[4 + 2 * j], acc0);
      if constexpr (ISSUE) {
        asm volatile("" : "+v"(acc0));
        ring[4 + 2 * j] = __builtin_amdgcn_raw_buffer_load_b128(rvb, vq[2 * j], vo, NT);
        FENCE();
      }
      acc1 = mfma(a, ring[5 + 2 * j], acc1);
      if constexpr (ISSUE) {
        asm volatile("" : "+v"(acc1));
        ring[5 + 2 * j] = __builtin_amdgcn_raw_buffer_load_b128(rvb, vq[2 * j + 1], vo, NT);
        FENCE();
      }
    }
  };
  if (cur < NCH && first < NCH) {
#pragma unroll 1
    do {
      process(std::true_type{});
      cur = claimed();
    } while (cur < NCH);
  }
  if (first < NCH) process(std::false_type{});

  park(1);
  const unsigned tag4 = tag << 4;
  WG_BARRIER();
  float ob;
  unsigned bbits;
  merge(1, ob, bbits);
  const u32x2 own = {pk_bf16(oa, ob), tag4 | abits | (bbits << 2)};
  __builtin_amdgcn_raw_buffer_store_b64(own, rw, dst1, 0, SC1);
  FENCE();
  {
    u32x2 v1;
    __builtin_amdgcn_s_sleep(8);
    for (unsigned spin = 0;; ++spin) {
      v1 = __builtin_amdgcn_raw_buffer_load_b64(rw, src1, 0, SC1);
      asm volatile("" ::: "memory");
      v1 = self_v ? own : v1;
      if (__all((v1[1] ^ tag4) < 16u) || spin > SPIN_CAP) break;
    }
    const u64 bma = __ballot(v1[1] & 1u), bla = __ballot(v1[1] & 2u), bmb = __ballot(v1[1] & 4u), blb = __ballot(v1[1] & 8u);
#define LO(b) __builtin_bit_cast(float, (unsigned)(b))
#define HI(b) __builtin_bit_cast(float, (unsigned)((b) >> 32))
    const bool lo = lane < 32;
    const float mxa = fmaxf(LO(bma), HI(bma)), mxb = fmaxf(LO(bmb), HI(bmb));
    const float ea = __builtin_amdgcn_exp2f((lo ? LO(bma) : HI(bma)) - mxa), eb = __builtin_amdgcn_exp2f((lo ? LO(bmb) : HI(bmb)) - mxb);
    const float cx = swap32_add(bf_lo(v1[0]) * ea, bf_hi(v1[0]) * eb);
    *pcv = (unsigned short)pk_bf16(cx, cx);
    const float l2 = swap32_add((lo ? LO(bla) : HI(bla)) * ea, (lo ? LO(blb) : HI(blb)) * eb);
    if ((lane & 31) == 0) { ml2[lane >> 5][0][wave] = lo ? mxa : mxb; ml2[lane >> 5][1][wave] = l2; }
  }
  WG_BARRIER();
  if (wave > 0) {
    const float mw = pml[0], lw = pml[NWAVE];
    const u32x4 aa = *paa, ab = *pab;
    FENCE();
    f32x4 d[4];
#pragma unroll
    for (int t = 0; t < 4; ++t) d[t] = mfma(aa, wo[t], f32x4{0.f, 0.f, 0.f, 0.f});
#pragma unroll
    for (int t = 0; t < 4; ++t) d[t] = mfma(ab, wo[4 + t], d[t]);
    const float sw = __builtin_amdgcn_exp2f(mw - max8(mw));
    const float wt = sw * gs * __builtin_amdgcn_rcpf(sum8(sw * lw));
    const float w0 = dpp<BCAST>(wt), w1 = dpp<BCAST + 1>(wt), w2 = dpp<BCAST + 2>(wt), w3 = dpp<BCAST + 3>(wt);
    float a[4];
#pragma unroll
    for (int t = 0; t < 4; ++t) a[t] = fmaf(d[t][3], w3, fmaf(d[t][2], w2, fmaf(d[t][1], w1, d[t][0] * w0)));
    const float y = swap16_add(swap32_add(a[0], a[2]), swap32_add(a[1], a[3]));
    __builtin_amdgcn_raw_buffer_store_b64(u32x2{__builtin_bit_cast(unsigned, y), tag}, rw, yoff, 0, SC1);
  } else if (bid < 224) {
    u32x2 v[8];
#pragma unroll
    for (int k = 0; k < 8; ++k) v[k] = u32x2{0u, ~tag};
    { unsigned unused = *(volatile gu32*)outp; asm volatile("" :: "v"(unused)); }
    __builtin_amdgcn_s_sleep(24);
    unsigned pend = 0xffu;
    for (unsigned spin = 0; pend != 0u && spin < SPIN_CAP; ++spin) {
#pragma unroll
      for (int k = 0; k < 8; ++k)
        if (pend >> k & 1u) v[k] = __builtin_amdgcn_raw_buffer_load_b64(rw, yoff, k * 32 * DM * 8, SC1);
      asm volatile("" ::: "memory");
      pend = 0u;
#pragma unroll
      for (int k = 0; k < 8; ++k) pend |= __all(v[k][1] == tag) ? 0u : 1u << k;
    }
#define GV(k) __builtin_bit_cast(float, v[k][0])
    float y = ((GV(0) + GV(1)) + (GV(2) + GV(3))) + ((GV(4) + GV(5)) + (GV(6) + GV(7)));
    y = swap32_add(y, y); y = swap16_add(y, y); y += dpp<XOR8>(y); y += xor4(y);
    const unsigned pr = pk_bf16(y, dpp<XOR1>(y));
    if ((lane & 61) == 0) __hip_atomic_store(outp, pr, RLX_AGENT);
    if (bid == 0 && lane == 0) __hip_atomic_store((gu32*)(p.ws + WS_EPOCH), tag, RLX_AGENT);
  }
}

extern "C" void kernel_launch(void* const* d_in, const int* in_sizes, int n_in, void* d_out, int out_size, void* d_ws, size_t ws_size,
                              hipStream_t stream) {
  Ptrs p;
  p.x = (const unsigned short*)d_in[0];
  p.k[0] = (const unsigned short*)d_in[1]; p.v[0] = (const unsigned short*)d_in[2];
  p.wq[0] = (const unsigned short*)d_in[3]; p.wo[0] = (const unsigned short*)d_in[4];
  p.k[1] = (const unsigned short*)d_in[6]; p.v[1] = (const unsigned short*)d_in[7];
  p.wq[1] = (const unsigned short*)d_in[8]; p.wo[1] = (const unsigned short*)d_in[9];
  p.gw = (const unsigned short*)d_in[10];
  p.out = (unsigned short*)d_out; p.ws = (unsigned char*)d_ws;
  gated_mix_decode_kernel<<<256, 512, 0, stream>>>(p);
}
```

```cpp
#include <hip/hip_runtime.h>
#include <hip/hip_bf16.h>
#include <stdint.h>
#include <utility>

typedef unsigned u32x4 __attribute__((ext_vector_type(4)));
typedef unsigned u32x2 __attribute__((ext_vector_type(2)));
typedef float f32x4 __attribute__((ext_vector_type(4)));
typedef float f32x2 __attribute__((ext_vector_type(2)));
typedef short bf16x8 __attribute__((ext_vector_type(8)));
typedef __bf16 bf16x2 __attribute__((ext_vector_type(2)));
typedef unsigned long long u64;
typedef const unsigned short cbf;
typedef __attribute__((address_space(1))) unsigned gu32;
typedef __attribute__((address_space(1))) const u32x4 gcu32x4;
#define RLX_AGENT __ATOMIC_RELAXED, __HIP_MEMORY_SCOPE_AGENT
#define RLX_WG __ATOMIC_RELAXED, __HIP_MEMORY_SCOPE_WORKGROUP

constexpr int DM = 448, HKV = 8, SK = 8192, DH = 256, NWAVE = 8;
constexpr int NS = 128;
constexpr int PRE = 20;
constexpr int LAG = 8;
constexpr int PACE = 452, POFF = 45;
constexpr unsigned SPIN_CAP = 1u << 18;

template <int... I, class F> __device__ __forceinline__ void static_for_impl(std::integer_sequence<int, I...>, F&& f) { (f(std::integral_constant<int, I>{}), ...); }
template <int N, class F> __device__ __forceinline__ void static_for(F&& f) { static_for_impl(std::make_integer_sequence<int, N>{}, f); }
template <int N> using IC = std::integral_constant<int, N>;

__device__ __forceinline__ float dot2(unsigned a, unsigned b, float acc) {
  return __builtin_amdgcn_fdot2_f32_bf16(__builtin_bit_cast(bf16x2, a), __builtin_bit_cast(bf16x2, b), acc, false);
}
__device__ __forceinline__ f32x4 mfma(u32x4 a, u32x4 b, f32x4 c) {
  return __builtin_amdgcn_mfma_f32_16x16x32_bf16(__builtin_bit_cast(bf16x8, a), __builtin_bit_cast(bf16x8, b), c, 0, 0, 0);
}
template <int CTRL> __device__ __forceinline__ float dpp(float x) {
  return __builtin_bit_cast(float, __builtin_amdgcn_mov_dpp(__builtin_bit_cast(int, x), CTRL, 0xf, 0xf, true));
}
constexpr int XOR1 = 0xB1, XOR2 = 0x4E, XOR3 = 0x1B, XOR7 = 0x141, XOR8 = 0x128, BCAST = 0x150;
__device__ __forceinline__ float bf_lo(unsigned u) { return __builtin_bit_cast(float, u << 16); }
__device__ __forceinline__ float bf_hi(unsigned u) { return __builtin_bit_cast(float, u & 0xffff0000u); }
__device__ __forceinline__ unsigned pk_bf16(float lo, float hi) {
  return __builtin_bit_cast(unsigned, __builtin_convertvector((f32x2){lo, hi}, bf16x2));
}
__device__ __forceinline__ float rbf(float f) { return bf_lo(pk_bf16(f, f)); }
__device__ __forceinline__ float swap16_add(float a, float b) {
  auto r = __builtin_amdgcn_permlane16_swap(__builtin_bit_cast(unsigned, a), __builtin_bit_cast(unsigned, b), false, false);
  return __builtin_bit_cast(float, (unsigned)r[0]) + __builtin_bit_cast(float, (unsigned)r[1]);
}
__device__ __forceinline__ float swap32_add(float a, float b) {
  auto r = __builtin_amdgcn_permlane32_swap(__builtin_bit_cast(unsigned, a), __builtin_bit_cast(unsigned, b), false, false);
  return __builtin_bit_cast(float, (unsigned)r[0]) + __builtin_bit_cast(float, (unsigned)r[1]);
}
__device__ __forceinline__ float xrow_max(float a) {
  auto r = __builtin_amdgcn_permlane16_swap(__builtin_bit_cast(unsigned, a), __builtin_bit_cast(unsigned, a), false, false);
  a = fmaxf(__builtin_bit_cast(float, (unsigned)r[0]), __builtin_bit_cast(float, (unsigned)r[1]));
  auto s = __builtin_amdgcn_permlane32_swap(__builtin_bit_cast(unsigned, a), __builtin_bit_cast(unsigned, a), false, false);
  return fmaxf(__builtin_bit_cast(float, (unsigned)s[0]), __builtin_bit_cast(float, (unsigned)s[1]));
}
__device__ __forceinline__ float xor4(float x) { return dpp<XOR3>(dpp<XOR7>(x)); }
__device__ __forceinline__ float sum8(float x) { x += dpp<XOR7>(x); x += dpp<XOR2>(x); return x + dpp<XOR1>(x); }
__device__ __forceinline__ float max8(float x) {
  float y;
  asm("s_nop 1\n\tv_max_f32_dpp %0, %1, %1 row_half_mirror row_mask:0xf bank_mask:0xf\n\ts_nop 1\n\t"
      "v_max_f32_dpp %0, %0, %0 quad_perm:[2,3,0,1] row_mask:0xf bank_mask:0xf\n\ts_nop 1\n\t"
      "v_max_f32_dpp %0, %0, %0 quad_perm:[1,0,3,2] row_mask:0xf bank_mask:0xf" : "=&v"(y) : "v"(x));
  return y;
}
#define WG_BARRIER() asm volatile("s_waitcnt lgkmcnt(0)\n\ts_barrier" ::: "memory")
#define INFLIGHT(n) asm volatile("s_waitcnt vmcnt(%0)" :: "n"((n) - 1))
#define FENCE() __builtin_amdgcn_sched_barrier(0)
#define RSRC(ptr, bytes) __builtin_amdgcn_make_buffer_rsrc((void*)(ptr), 0, (bytes), 0x00020000)

constexpr int WS_EPOCH = 0;
constexpr int WS_QG = 4096;
constexpr int WS_SG = WS_QG + 16 * 512 * 8;
constexpr int WS_OG = WS_SG + 256 * 128;
constexpr int WS_YG = WS_OG + 256 * 512 * 8;
constexpr int WS_END = WS_YG + 256 * DM * 8;
constexpr int SC1 = 16, NT = 2, OOB = 0x7ffffff0;

__global__ void __launch_bounds__(512, 2) gated_mix_decode_kernel(
    cbf* k_a, cbf* wq_a, cbf* wq_b, cbf* x, unsigned char* ws, cbf* wo_a, cbf* wo_b, cbf* gate_w, cbf* v_a, cbf* k_b, cbf* v_b,
    unsigned short* out) {
  __shared__ __attribute__((aligned(16))) unsigned q_l[512];
  __shared__ __attribute__((aligned(16))) float ol[2][NWAVE][512];
  __shared__ __attribute__((aligned(16))) float ml[2][4][NWAVE];
  __shared__ __attribute__((aligned(16))) unsigned short cvb[2][16][32];
  __shared__ __attribute__((aligned(16))) float gst[2][4];
  __shared__ __attribute__((aligned(16))) unsigned misc[8];

  const int bid = blockIdx.x;
  const int s = bid & 15, b = (bid >> 4) & 1, kvh = bid >> 5, grp = bid >> 4;
  const int tid = threadIdx.x, lane = tid & 63;
  const int wave = __builtin_amdgcn_readfirstlane(tid >> 6);
  unsigned tstart;

  const size_t base = (size_t)(b * HKV + kvh) * SK * DH;
  const __amdgpu_buffer_rsrc_t rka = RSRC(k_a + base, SK * DH * 2), rva = RSRC(v_a + base, SK * DH * 2);
  const __amdgpu_buffer_rsrc_t rkb = RSRC(k_b + base, SK * DH * 2), rvb = RSRC(v_b + base, SK * DH * 2);
  const __amdgpu_buffer_rsrc_t rw = RSRC(ws, WS_END);
  const int r8 = wave & 1;
  int vq[4];
#pragma unroll
  for (int i = 0; i < 4; ++i) vq[i] = (i >> 1) * 2048 + (lane >> 4) * 512 + ((i & 1) ^ r8) * 256 + (lane & 15) * 16;
  const int sto = (((s >> 1) << 7) | ((s & 1) << 3) | (wave ^ ((s ^ grp) & 7))) << 12;
  u32x4 buf[NS], wo[8];
  auto issue = [&](auto T) {
    constexpr int t = decltype(T)::value;
    if constexpr (t >= 8 && t < NS - 2) {
      while ((int)((unsigned)__builtin_amdgcn_s_memrealtime() - tstart) < POFF + t * PACE / 16) __builtin_amdgcn_s_sleep(1);
      FENCE();
    }
    buf[t] = __builtin_amdgcn_raw_buffer_load_b128(t < 32 ? rka : t < 64 ? rva : t < 96 ? rkb : rvb, vq[t & 3], sto + ((t & 31) >> 2) * 65536, NT);
    FENCE();
  };

  if (wave == 0) {
    const int u = lane >> 2, c4 = lane & 3, jq = ((s & 7) << 1) | (s >> 3);
    const __amdgpu_buffer_rsrc_t rq = RSRC((b ? wq_b : wq_a) + kvh * 512 + jq * 32, 447 * 8192 + 64);
    const __amdgpu_buffer_rsrc_t rx = RSRC(x, 2 * DM * 2), rg = RSRC(gate_w, DM * 2);
    const unsigned epoch = __builtin_amdgcn_raw_buffer_load_b32(rw, WS_EPOCH, 0, SC1);
    u32x4 xv[3][4], w[28];
#pragma unroll
    for (int i = 0; i < 4; ++i) {
      xv[0][i] = __builtin_amdgcn_raw_buffer_load_b128(rx, 56 * u, 16 * i, 0);
      xv[1][i] = __builtin_amdgcn_raw_buffer_load_b128(rx, 56 * u, DM * 2 + 16 * i, 0);
    }
#pragma unroll
    for (int i = 0; i < 28; ++i) w[i] = __builtin_amdgcn_raw_buffer_load_b128(rq, u * (28 * 8192) + c4 * 16, i * 8192, NT);
#pragma unroll
    for (int i = 0; i < 4; ++i) xv[2][i] = __builtin_amdgcn_raw_buffer_load_b128(rg, 56 * u, 16 * i, 0);
    FENCE();
    asm volatile("s_barrier" ::: "memory");
    tstart = (unsigned)__builtin_amdgcn_s_memrealtime();
    FENCE();
    const unsigned tag = __builtin_amdgcn_readfirstlane(epoch) + 1u;
    float a[16];
#pragma unroll
    for (int e = 0; e < 16; ++e) a[e] = 0.f;
#pragma unroll
    for (int i = 0; i < 14; ++i)
#pragma unroll
      for (int e = 0; e < 4; ++e) {
        const unsigned lo = __builtin_amdgcn_perm(w[2 * i + 1][e], w[2 * i][e], 0x05040100u);
        const unsigned hi = __builtin_amdgcn_perm(w[2 * i + 1][e], w[2 * i][e], 0x07060302u);
        const unsigned x0 = xv[0][i >> 2][i & 3], x1 = xv[1][i >> 2][i & 3];
        a[2 * e] = dot2(lo, x0, a[2 * e]); a[2 * e + 1] = dot2(hi, x0, a[2 * e + 1]);
        a[8 + 2 * e] = dot2(lo, x1, a[8 + 2 * e]); a[8 + 2 * e + 1] = dot2(hi, x1, a[8 + 2 * e + 1]);
      }
#pragma unroll
    for (int e = 0; e < 8; ++e) a[e] = swap32_add(a[e], a[e + 8]);
#pragma unroll
    for (int e = 0; e < 4; ++e) a[e] = swap16_add(a[e], a[e + 4]);
#pragma unroll
    for (int e = 0; e < 2; ++e) {
      const float t1 = a[e] + dpp<XOR8>(a[e]), t2 = a[e + 2] + dpp<XOR8>(a[e + 2]);
      a[e] = (lane & 8) ? t2 : t1;
      a[e] += xor4(a[e]);
    }
    __builtin_amdgcn_raw_buffer_store_b64(u32x2{pk_bf16(a[0], a[1]), tag}, rw,
        (lane & 4) ? OOB : WS_QG + (((kvh * 2 + (lane >> 5)) * 2 + b) * 256 + jq * 16 + c4 * 4 + ((lane >> 4) & 1) * 2 + ((lane >> 3) & 1)) * 8, 0, SC1);
    float g = 0.f;
#pragma unroll
    for (int i = 0; i < 14; ++i) g = dot2(b ? xv[1][i >> 2][i & 3] : xv[0][i >> 2][i & 3], xv[2][i >> 2][i & 3], g);
    g = swap32_add(g, g); g = swap16_add(g, g); g += dpp<XOR8>(g); g += xor4(g);
    g = rbf(1.f / (1.f + __expf(-rbf(g))));
    if (lane == 0) { misc[0] = tag; misc[1] = __builtin_bit_cast(unsigned, g); misc[2] = __builtin_bit_cast(unsigned, rbf(1.f - g)); }
    static_for<8>([&](auto T) { if (decltype(T)::value >= 4) INFLIGHT(4); issue(T); });
    for (unsigned spin = 0;; ++spin) {
      bool ok = true;
      u32x2 v[8];
#pragma unroll
      for (int k = 0; k < 8; ++k) v[k] = __builtin_amdgcn_raw_buffer_load_b64(rw, WS_QG + (grp * 512 + lane) * 8, 512 * k, SC1);
      asm volatile("" ::: "memory");
#pragma unroll
      for (int k = 0; k < 8; ++k) { q_l[lane + 64 * k] = v[k][0]; ok &= v[k][1] == tag; }
      if (__all(ok) || spin > SPIN_CAP) break;
      __builtin_amdgcn_s_sleep(1);
    }
    asm volatile("s_waitcnt lgkmcnt(0)" ::: "memory");
    if (lane == 0) __hip_atomic_store(&misc[3], 1u, RLX_WG);
    static_for<PRE - 8>([&](auto T) { if (decltype(T)::value >= 6) INFLIGHT(4); issue(IC<decltype(T)::value + 8>{}); });
#pragma unroll
    for (int i = 0; i < 8; ++i) wo[i] = u32x4{0u, 0u, 0u, 0u};
  } else {
    gcu32x4* wop = (gcu32x4*)(((lane & 8) ? wo_b : wo_a) + (size_t)(kvh * 512 + s * 32 + (lane >> 4) * 8) * DM + (wave - 1) * 64 + (lane & 7) * 8);
    if (tid < 64 + 4) misc[3 + lane] = 0u;
    static_for<2>([&](auto T) { issue(T); });
    WG_BARRIER();
    const u64 t_bar = __builtin_amdgcn_s_memrealtime();
    FENCE();
    u32x4 w[8];
#pragma unroll
    for (int i = 0; i < 8; ++i) {
      if (i == 6) tstart = (unsigned)t_bar + (unsigned)(wave * PACE / 128);
      if (i >= 6) INFLIGHT(4);
      w[i] = __builtin_nontemporal_load(wop + i * (DM / 8));
      FENCE();
    }
    static_for<PRE - 2>([&](auto T) { INFLIGHT(4); issue(IC<decltype(T)::value + 2>{}); });
    const bool up = lane & 8;
    unsigned h[2][8][2];
#pragma unroll
    for (int i = 0; i < 8; ++i)
#pragma unroll
      for (int e = 0; e < 2; ++e) {
        const unsigned recv = __builtin_amdgcn_mov_dpp(up ? w[i][e] : w[i][2 + e], XOR8, 0xf, 0xf, true);
        h[0][i][e] = up ? recv : w[i][e];
        h[1][i][e] = up ? w[i][2 + e] : recv;
      }
#pragma unroll
    for (int br = 0; br < 2; ++br)
#pragma unroll
      for (int i = 0; i < 4; ++i)
#pragma unroll
        for (int e = 0; e < 2; ++e) {
          wo[4 * br + 2 * e][i] = __builtin_amdgcn_perm(h[br][2 * i + 1][e], h[br][2 * i][e], 0x05040100u);
          wo[4 * br + 2 * e + 1][i] = __builtin_amdgcn_perm(h[br][2 * i + 1][e], h[br][2 * i][e], 0x07060302u);
        }
#pragma unroll
    for (int e = 0; e < 8; ++e) asm volatile("" : "+v"(wo[e]));
    while (__hip_atomic_load(&misc[3], RLX_WG) == 0u) __builtin_amdgcn_s_sleep(1);
    asm volatile("" ::: "memory");
  }
  FENCE();
  const unsigned tag = misc[0];
  u32x4 qa[2], qb[2];
  auto load_q = [&](int br) {
    const int h = (lane >> 3) & 1, c = lane & 15;
#pragma unroll
    for (int hf = 0; hf < 2; ++hf) {
      qa[hf] = *(const u32x4*)&q_l[br * 256 + h * 128 + (hf ^ r8) * 64 + 4 * c];
      qb[hf] = *(const u32x4*)&q_l[br * 256 + (h ^ 1) * 128 + (hf ^ r8) * 64 + 4 * c];
    }
  };
  load_q(0);
  unsigned msk[4];
#pragma unroll
  for (int r = 0; r < 4; ++r) msk[r] = (((lane & 7) >> 1) == r) ? ((lane & 1) ? 0xffff0000u : 0x0000ffffu) : 0u;
  constexpr float C2 = 0.0625f * 1.4426950408889634f;
  float m = -INFINITY;
  f32x4 acc0 = {0.f, 0.f, 0.f, 0.f}, acc1 = {0.f, 0.f, 0.f, 0.f};

  typedef __attribute__((address_space(3))) const u32x4 lu32x4;
  typedef __attribute__((address_space(3))) float lf32w;
  typedef __attribute__((address_space(3))) f32x4 lf32x4w;
  typedef __attribute__((address_space(3))) unsigned short lu16;
  const bool self_v = (tid >> 5) == s;
  int src1 = self_v ? OOB : WS_OG + (((grp * 16 + (tid >> 5)) * 16 + s) * 32 + (lane & 31)) * 8;
  int dst1 = self_v ? OOB : WS_OG + (bid * 512 + tid) * 8;
  const int yl = (bid >> 5) * 8 + (bid & 7);
  const int fb = yl >= 28 ? 1 : 0, cb = (yl - fb * 28) * 4 + ((bid >> 3) & 3);
  int yoff = wave > 0 ? WS_YG + (bid * DM + (wave - 1) * 64 + (lane & 7) * 8 + ((lane >> 3) & 1) * 4 + (lane >> 4)) * 8
                      : WS_YG + ((fb * 16 + (lane >> 2)) * DM + cb * 4 + (lane & 3)) * 8;
  gu32* outp = (gu32*)(out + fb * DM + cb * 4 + (lane & 2));
  lu32x4* paa = (lu32x4*)&cvb[0][lane & 15][(lane >> 4) * 8];
  lu16* pcv = (lu16*)&cvb[0][tid >> 5][lane & 31];
  lf32w* pol = (lf32w*)&ol[1][wave][(lane >> 5) * 256 + 8 * (lane & 15) + ((lane >> 4) & 1) * 4];
  asm volatile("" : "+v"(src1), "+v"(dst1), "+v"(yoff), "+v"(outp));
  asm volatile("" : "+v"(paa), "+v"(pcv), "+v"(pol));

  auto p_operand = [&](float sc, float mc) {
    const float pv = __builtin_amdgcn_exp2f(fmaf(sc, C2, -mc));
    const unsigned pd = pk_bf16(pv, pv);
    return u32x4{pd & msk[0], pd & msk[1], pd & msk[2], pd & msk[3]};
  };

  auto lds_wait = [&](int word, unsigned n) { while (__hip_atomic_load(&misc[word], RLX_WG) < n) __builtin_amdgcn_s_sleep(1); asm volatile("" ::: "memory"); };
  auto lds_post = [&](int word) { asm volatile("s_waitcnt lgkmcnt(0)" ::: "memory"); if (lane == 0) __hip_atomic_fetch_add(&misc[word], 1u, RLX_WG); };
  float oa = 0.f, wb = 0.f, xa = 0.f, xb = 0.f, s0 = 0.f, mc = 0.f;
  unsigned sp[8];
  u32x4 pa;
  auto consume = [&](auto I) {
    constexpr int i = decltype(I)::value;
    constexpr bool isK = (i & 32) == 0;
    constexpr int r = i & 31;
    if constexpr (i == 64) {
      float* olw = &ol[0][wave][(lane >> 5) * 256 + 8 * (lane & 15) + ((lane >> 4) & 1) * 4];
      *(f32x4*)(olw + r8 * 128) = acc0;
      *(f32x4*)(olw + (r8 ^ 1) * 128) = acc1;
      load_q(1);
      m = -INFINITY; acc0 = f32x4{0.f, 0.f, 0.f, 0.f}; acc1 = acc0;
    }
    if constexpr (i == 96 + 4) {
      if (wave == 7) {
        lds_wait(4, 8u);
        const float mw = ml[lane >> 5][(lane >> 4) & 1][lane & 7], lw = ml[lane >> 5][2 + ((lane >> 4) & 1)][lane & 7];
        const float M = max8(mw), L = sum8(__builtin_amdgcn_exp2f(mw - M) * lw);
        __builtin_amdgcn_raw_buffer_store_b64(u32x2{__builtin_bit_cast(unsigned, (lane & 8) ? L : M), tag}, rw, (lane & 7) ? OOB : WS_SG + bid * 128 + lane, 0, SC1);
      }
    }
    if constexpr (i == 96 + 14) {
      if (wave == 6) {
        u32x4 v;
        for (unsigned spin = 0;; ++spin) {
          const u32x2 v0 = __builtin_amdgcn_raw_buffer_load_b64(rw, WS_SG + (grp * 16 + (lane & 15)) * 128 + (lane >> 4) * 16, 0, SC1);
          const u32x2 v1 = __builtin_amdgcn_raw_buffer_load_b64(rw, WS_SG + (grp * 16 + (lane & 15)) * 128 + (lane >> 4) * 16, 8, SC1);
          asm volatile("" ::: "memory");
          v = u32x4{v0[0], v0[1], v1[0], v1[1]};
          if (__all((v[1] == tag) & (v[3] == tag)) || spin > SPIN_CAP) break;
          __builtin_amdgcn_s_sleep(2);
        }
        const float mp = __builtin_bit_cast(float, (unsigned)v[0]);
        float M = max8(mp); M = fmaxf(M, dpp<XOR8>(M));
        float L = sum8(__builtin_amdgcn_exp2f(mp - M) * __builtin_bit_cast(float, (unsigned)v[2])); L += dpp<XOR8>(L);
        if ((lane & 15) == 0) { gst[0][lane >> 4] = M; gst[1][lane >> 4] = __builtin_bit_cast(float, misc[1 + (lane >> 5)]) / L; }
        lds_post(5);
      }
    }
    if constexpr (i == 96 + 20) {
      lds_wait(5, 1u);
      const float sw = __builtin_amdgcn_exp2f(ml[0][wave >> 2][lane & 7] - gst[0][wave >> 2]) * gst[1][wave >> 2];
      oa = dpp<BCAST>(sw) * ol[0][0][tid];
      static_for<7>([&](auto W) { constexpr int w = decltype(W)::value + 1; oa = fmaf(dpp<BCAST + w>(sw), ol[0][w][tid], oa); });
      wb = __builtin_amdgcn_exp2f(ml[1][lane >> 5][wave] - gst[0][2 + (lane >> 5)]) * gst[1][2 + (lane >> 5)];
      asm volatile("" : "+v"(oa), "+v"(wb));
    }
    if constexpr (isK) {
      constexpr int hf = r & 1;
      if constexpr (hf == 0) { xa = 0.f; xb = 0.f; }
#pragma unroll
      for (int e = 0; e < 4; ++e) { xa = dot2(buf[i][e], qa[hf][e], xa); xb = dot2(buf[i][e], qb[hf][e], xb); }
      if constexpr (hf == 1) {
        const float y = sum8(xa + dpp<XOR8>(xb));
        if constexpr ((r & 2) == 0) { s0 = y; asm volatile("" : "+v"(s0)); }
        else {
          sp[r >> 2] = pk_bf16(s0, y);
          m = fmaxf(m, fmaxf(bf_lo(sp[r >> 2]), bf_hi(sp[r >> 2])));
          asm volatile("" : "+v"(m));
        }
      } else asm volatile("" : "+v"(xa), "+v"(xb));
    } else {
      if constexpr (r == 0) {
        m = xrow_max(m); mc = m * C2;
        float l = 0.f;
#pragma unroll
        for (int c = 0; c < 8; ++c) l += __builtin_amdgcn_exp2f(fmaf(bf_lo(sp[c]), C2, -mc)) + __builtin_amdgcn_exp2f(fmaf(bf_hi(sp[c]), C2, -mc));
        l = swap16_add(l, l); l = swap32_add(l, l);
        if ((lane & 55) == 0) { ml[i >> 6][lane >> 3][wave] = mc; ml[i >> 6][2 + (lane >> 3)][wave] = l; }
        if constexpr (i == 96) lds_post(4);
#pragma unroll
        for (int c = 0; c < 8; ++c) asm volatile("" : "+v"(sp[c]));
      }
      if constexpr ((r & 1) == 0) {
        pa = p_operand((r & 2) ? bf_hi(sp[r >> 2]) : bf_lo(sp[r >> 2]), mc);
        acc0 = mfma(pa, buf[i], acc0);
        asm volatile("" : "+v"(acc0));
      } else {
        acc1 = mfma(pa, buf[i], acc1);
        asm volatile("" : "+v"(acc1));
      }
    }
  };
  static_for<NS - PRE>([&](auto U) {
    constexpr int t = decltype(U)::value + PRE;
    constexpr int c0 = 2 * (t - PRE) < t - LAG ? 2 * (t - PRE) : t - LAG;
    constexpr int c1 = 2 * (t - PRE + 1) < t + 1 - LAG ? 2 * (t - PRE + 1) : t + 1 - LAG;
    static_for<c1 - c0>([&](auto K) { consume(IC<c0 + decltype(K)::value>{}); });
    if constexpr (t < 32) INFLIGHT(4); else if constexpr (t < 96) INFLIGHT(3); else if constexpr (t < NS - 2) INFLIGHT(5);
    issue(IC<t>{});
  });
  static_for<LAG>([&](auto K) { consume(IC<NS - LAG + decltype(K)::value>{}); });

  *(lf32x4w*)(pol + r8 * 128) = acc0 * wb;
  *(lf32x4w*)(pol + (r8 ^ 1) * 128) = acc1 * wb;
  WG_BARRIER();
  const float ob = ((ol[1][0][tid] + ol[1][1][tid]) + (ol[1][2][tid] + ol[1][3][tid])) + ((ol[1][4][tid] + ol[1][5][tid]) + (ol[1][6][tid] + ol[1][7][tid]));
  const u32x2 own = {pk_bf16(oa, ob), tag};
  __builtin_amdgcn_raw_buffer_store_b64(own, rw, dst1, 0, SC1);
  FENCE();
  {
    u32x2 v1;
    __builtin_amdgcn_s_sleep(12);
    for (unsigned spin = 0;; ++spin) {
      v1 = __builtin_amdgcn_raw_buffer_load_b64(rw, src1, 0, SC1);
      asm volatile("" ::: "memory");
      v1 = self_v ? own : v1;
      if (__all(v1[1] == tag) || spin > SPIN_CAP) break;
    }
    pcv[0] = (unsigned short)v1[0];
    pcv[512] = (unsigned short)(v1[0] >> 16);
  }
  WG_BARRIER();
  if (wave > 0) {
    const u32x4 aa = paa[0], ab = paa[64];
    FENCE();
    f32x4 d[4];
#pragma unroll
    for (int t = 0; t < 4; ++t) d[t] = mfma(aa, wo[t], f32x4{0.f, 0.f, 0.f, 0.f});
#pragma unroll
    for (int t = 0; t < 4; ++t) d[t] = mfma(ab, wo[4 + t], d[t]);
    float a[4];
#pragma unroll
    for (int t = 0; t < 4; ++t) a[t] = (d[t][0] + d[t][1]) + (d[t][2] + d[t][3]);
    const float y = swap16_add(swap32_add(a[0], a[2]), swap32_add(a[1], a[3]));
    __builtin_amdgcn_raw_buffer_store_b64(u32x2{__builtin_bit_cast(unsigned, y), tag}, rw, yoff, 0, SC1);
  } else if (bid < 224) {
    u32x2 v[8];
#pragma unroll
    for (int k = 0; k < 8; ++k) v[k] = u32x2{0u, ~tag};
    { unsigned unused = *(volatile gu32*)outp; asm volatile("" :: "v"(unused)); }
    __builtin_amdgcn_s_sleep(19);
    unsigned pend = 0xffu;
    for (unsigned spin = 0; pend != 0u && spin < SPIN_CAP; ++spin) {
#pragma unroll
      for (int k = 0; k < 8; ++k)
        if (pend >> k & 1u) v[k] = __builtin_amdgcn_raw_buffer_load_b64(rw, yoff, k * 32 * DM * 8, SC1);
      asm volatile("" ::: "memory");
      pend = 0u;
#pragma unroll
      for (int k = 0; k < 8; ++k) pend |= __all(v[k][1] == tag) ? 0u : 1u << k;
    }
#define GV(k) __builtin_bit_cast(float, v[k][0])
    float y = ((GV(0) + GV(1)) + (GV(2) + GV(3))) + ((GV(4) + GV(5)) + (GV(6) + GV(7)));
    y = swap32_add(y, y); y = swap16_add(y, y); y += dpp<XOR8>(y); y += xor4(y);
    const unsigned pr = pk_bf16(y, dpp<XOR1>(y));
    if ((lane & 61) == 0) __hip_atomic_store(outp, pr, RLX_AGENT);
    if (bid == 0 && lane == 0) __hip_atomic_store((gu32*)(ws + WS_EPOCH), tag, RLX_AGENT);
  }
}

extern "C" void kernel_launch(void* const* d_in, const int* in_sizes, int n_in, void* d_out, int out_size, void* d_ws, size_t ws_size,
                              hipStream_t stream) {
  gated_mix_decode_kernel<<<256, 512, 0, stream>>>((cbf*)d_in[1], (cbf*)d_in[3], (cbf*)d_in[8], (cbf*)d_in[0], (unsigned char*)d_ws,
                                                   (cbf*)d_in[4], (cbf*)d_in[9], (cbf*)d_in[10], (cbf*)d_in[2], (cbf*)d_in[6],
                                                   (cbf*)d_in[7], (unsigned short*)d_out);
}
```
